```python
import torch

def _softmax(x, axis=-1):
    return torch.softmax(x, dim=axis)
def _gelu(x):
    return torch.nn.functional.gelu(x, approximate="tanh")
def _silu(x):
    return torch.nn.functional.silu(x)
def _sigmoid(x):
    return torch.sigmoid(x)
def _layernorm(x, axis=-1, eps=1e-5):
    m = x.mean(dim=axis, keepdim=True)
    v = x.var(dim=axis, keepdim=True, unbiased=False)
    return (x - m) / torch.sqrt(v + eps)
def _scatter_add(tgt, idx, src, axis=0):
    return tgt.clone().index_add_(axis, idx, src)
def _segment_sum(src, seg, n):
    out = torch.zeros((n,) + src.shape[1:], dtype=src.dtype)
    return out.index_add_(0, seg, src)
def _pad(x, pw):
    flat = tuple(v for pair in reversed(pw) for v in pair)
    return torch.nn.functional.pad(x, flat)

kernel_name = 'sequential_preLN__sliding_local_dilated__geglu_ffn__prompt'

def setup_inputs():

    _w = (lambda a: (lambda t: t.to(torch.bfloat16) if t.is_floating_point() else t)(torch.as_tensor(a)))
    def _A_setup():
        B, S, D = 1, 2048, 512
        return {
            "x": _w((lambda *s: torch.randn(*s, dtype=torch.bfloat16) * 0.05)(B, S, D)),
            "wqkv": _w((lambda *s: torch.randn(*s, dtype=torch.bfloat16) * 0.05)(D, 3 * D)),
            "wo": _w((lambda *s: torch.randn(*s, dtype=torch.bfloat16) * 0.05)(D, D)),
        }
    _A_in = _A_setup()

    def _B_setup():
        B, S, D, F = 1, 2048, 512, 1792
        return {
            "x": _w((lambda *s: torch.randn(*s, dtype=torch.bfloat16) * 0.05)(B, S, D)),
            "w_gate": _w((lambda *s: torch.randn(*s, dtype=torch.bfloat16) * 0.05)(D, F)),
            "w_up": _w((lambda *s: torch.randn(*s, dtype=torch.bfloat16) * 0.05)(D, F)),
            "w_down": _w((lambda *s: torch.randn(*s, dtype=torch.bfloat16) * 0.05)(F, D)),
        }
    _B_in = _B_setup()

    return {
        "A_x": _A_in["x"],
        "A_wqkv": _A_in["wqkv"],
        "A_wo": _A_in["wo"],
        "B_x": _B_in["x"],
        "B_w_gate": _B_in["w_gate"],
        "B_w_up": _B_in["w_up"],
        "B_w_down": _B_in["w_down"],
    }

def reference(A_x, A_wqkv, A_wo, B_x, B_w_gate, B_w_up, B_w_down):
    def _A_ref(x, wqkv, wo):
        B, S, D = x.shape
        H = 32; Dh = D // H
        W, dil = 8, 2
        qkv = (lambda x, *d: x.permute(*d))((x @ wqkv).reshape(B, S, 3, H, Dh), 2, 0, 3, 1, 4)
        q, k, v = qkv[0], qkv[1], qkv[2]
        scale = Dh ** -0.5
        pos = torch.arange(S)
        off = torch.arange(W) * dil
        idx = pos[:, None] - off[None, :]
        mask = idx >= 0
        idx_c = torch.where(mask, idx, 0).reshape(-1)
        kg = k[:, :, idx_c, :].reshape(B, H, S, W, Dh)
        vg = v[:, :, idx_c, :].reshape(B, H, S, W, Dh)
        logits = torch.einsum("bhsd,bhswd->bhsw", q, kg) * scale
        logits = torch.where(mask[None, None, :, :], logits, -1e9)
        attn = _softmax(logits, axis=-1)
        o = torch.einsum("bhsw,bhswd->bhsd", attn, vg)
        return (lambda x, *d: x.permute(*d))(o, 0, 2, 1, 3).reshape(B, S, D) @ wo

    def _B_ref(x, w_gate, w_up, w_down):
        return (_gelu(x @ w_gate) * (x @ w_up)) @ w_down

    x = A_x
    h = x + _A_ref(_layernorm(x, axis=-1), A_wqkv, A_wo)
    h2 = h + _B_ref(_layernorm(h, axis=-1), B_w_gate, B_w_up, B_w_down)
    return h2
```

```cpp
#include <hip/hip_runtime.h>
#include <hip/hip_bf16.h>

#define GAS __attribute__((address_space(1)))
#define LAS __attribute__((address_space(3)))
typedef short bf16x8 __attribute__((ext_vector_type(8)));
typedef short bf16x4 __attribute__((ext_vector_type(4)));
typedef __bf16 bf2 __attribute__((ext_vector_type(2)));
typedef float f32x4 __attribute__((ext_vector_type(4)));
typedef float f32x2 __attribute__((ext_vector_type(2)));
typedef unsigned v4u __attribute__((ext_vector_type(4)));
typedef unsigned v2u __attribute__((ext_vector_type(2)));
#define DI __device__ __forceinline__

constexpr int S_ = 2048, D_ = 512, F_ = 1792;
constexpr int AUX_SC1 = 16;
constexpr unsigned RSRC3 = 0x00020000u;
constexpr unsigned WS_ATT = 0u;
constexpr unsigned WS_HF = 2u << 20;
constexpr unsigned WS_ACT = 4u << 20;
constexpr unsigned WS_ST = 12u << 20;
constexpr unsigned WS_BYTES = 13u << 20;

struct Params {
  const unsigned short* x;
  const unsigned short* wqkv;
  const unsigned short* wo;
  const unsigned short* wg;
  const unsigned short* wu;
  const unsigned short* wd;
  unsigned short* out;
  unsigned char* ws;
};

DI float asf(unsigned u) { return __builtin_bit_cast(float, u); }
DI unsigned asu(float f) { return __builtin_bit_cast(unsigned, f); }
DI f32x4 mfma32(bf16x8 a, bf16x8 b, f32x4 c) { return __builtin_amdgcn_mfma_f32_16x16x32_bf16(a, b, c, 0, 0, 0); }
DI f32x4 mfma16(bf16x4 a, bf16x4 b, f32x4 c) { return __builtin_amdgcn_mfma_f32_16x16x16bf16_1k(a, b, c, 0, 0, 0); }
DI unsigned pack2(float lo, float hi) { bf2 p; p[0] = (__bf16)lo; p[1] = (__bf16)hi; return __builtin_bit_cast(unsigned, p); }
DI float bflo(unsigned u) { return __builtin_bit_cast(float, u << 16); }
DI float bfhi(unsigned u) { return __builtin_bit_cast(float, u & 0xffff0000u); }
DI unsigned ones2() { unsigned o = 0x3F803F80u; asm volatile("" : "+v"(o)); return o; }
DI float dot2(unsigned a, unsigned b, float c) { return __builtin_amdgcn_fdot2_f32_bf16(__builtin_bit_cast(bf2, a), __builtin_bit_cast(bf2, b), c, false); }
DI __amdgpu_buffer_rsrc_t mkrs(const void* p, unsigned bytes) { return __builtin_amdgcn_make_buffer_rsrc((void*)p, 0, bytes, RSRC3); }
DI void dma16(__amdgpu_buffer_rsrc_t rs, LAS unsigned char* dst, unsigned voff, unsigned soff) {
  asm volatile("s_nop 4\n\ts_mov_b32 m0, %0\n\ts_nop 0\n\tbuffer_load_dwordx4 %1, %2, %3 offen lds"
               :: "s"(__builtin_amdgcn_readfirstlane((unsigned)(size_t)dst)), "v"(voff), "s"(rs), "s"(__builtin_amdgcn_readfirstlane(soff)) : "m0", "memory");
}
DI bf16x8 lds_frag(LAS unsigned char* p) { return *(LAS bf16x8*)p; }
DI bf16x8 tr_frag(LAS unsigned char* p) {
  bf16x4 lo = __builtin_amdgcn_ds_read_tr16_b64_v4i16((LAS bf16x4*)p);
  bf16x4 hi = __builtin_amdgcn_ds_read_tr16_b64_v4i16((LAS bf16x4*)(p + 512));
  return __builtin_shufflevector(lo, hi, 0, 1, 2, 3, 4, 5, 6, 7);
}
DI void swap16(float v, float& a, float& b) {
  const unsigned u = asu(v);
  auto r = __builtin_amdgcn_permlane16_swap(u, u, false, false);
  a = asf(r[0]); b = asf(r[1]);
}
DI void swap32(float v, float& a, float& b) {
  const unsigned u = asu(v);
  auto r = __builtin_amdgcn_permlane32_swap(u, u, false, false);
  a = asf(r[0]); b = asf(r[1]);
}
DI float xsum_g(float v) {
  float a, b;
  swap16(v, a, b); v = a + b;
  swap32(v, a, b); return a + b;
}
DI float xmax_g(float v) {
  float a, b;
  swap16(v, a, b); v = fmaxf(a, b);
  swap32(v, a, b); return fmaxf(a, b);
}
#define WAITV(n) asm volatile("s_waitcnt vmcnt(" #n ")" ::: "memory")
#define WAITL0() asm volatile("s_waitcnt lgkmcnt(0)" ::: "memory")
#define BAR() asm volatile("s_barrier" ::: "memory")
constexpr float LN_EPS = 1e-5f;

DI unsigned wtr_lane(int lane, int nt) {
  const int g = lane >> 4, q = (lane & 15) >> 2, p = lane & 3;
  const int sg = (q >> 1) | ((g & 1) << 1);
  return g * 1024 + q * 128 + ((nt ^ sg) << 5) + p * 8;
}
DI unsigned wdma_lane(int lane, unsigned pitch, int odd) {
  const int r8 = lane >> 3, pos = lane & 7;
  return r8 * pitch + ((pos ^ (((r8 >> 1) & 1) << 1) ^ (odd << 2)) << 4);
}

constexpr int P1_X = 0;
constexpr int P1_W = 81920;
constexpr int P1_SM = P1_W + 3 * 24576;
DI void phase1(const Params& p, LAS unsigned char* L, int rt, int hg, int rot) {
  const int tid = threadIdx.x, lane = tid & 63, w = __builtin_amdgcn_readfirstlane(tid >> 6), hh = w & 3, rh = w >> 2;
  const __amdgpu_buffer_rsrc_t rx = mkrs(p.x, S_ * D_ * 2), rw = mkrs(p.wqkv, D_ * 3 * D_ * 2), rws = mkrs(p.ws, WS_BYTES);
  const int r8 = lane >> 3, pos = lane & 7;
  const int xce = pos ^ (r8 >> 1);
  const int rowb = 64 * rt - 16 + r8;
  auto issue_x = [&](int half) {
#pragma unroll
    for (int i = 0; i < 5; ++i) {
      const int idx = half * 40 + w * 5 + i, c = idx / 10, P = idx % 10;
      int row = rowb + 8 * P;
      row = row < 0 ? 0 : row;
      const unsigned voff = row * 1024 + ((xce ^ ((P & 1) << 2)) << 4);
      dma16(rx, L + P1_X + idx * 1024, voff, ((c + rot) & 7) * 128);
    }
  };
  const unsigned wl0 = wdma_lane(lane, 3072, 0), wl1 = wdma_lane(lane, 3072, 1);
  auto issue_w = [&](int c) {
#pragma unroll
    for (int i = 0; i < 3; ++i) {
      const int idx = w * 3 + i, part = idx >> 3, P = idx & 7;
      dma16(rw, L + P1_W + (c % 3) * 24576 + idx * 1024, (P & 1) ? wl1 : wl0, (((c + rot) & 7) * 64 + 8 * P) * 3072 + part * 1024 + hg * 128);
    }
  };
  issue_x(0); issue_w(0); issue_w(1); issue_w(2); issue_x(1);

  const f32x4 z4 = {0.f, 0.f, 0.f, 0.f};
  f32x4 aq[2] = {z4, z4}, ak[3] = {z4, z4, z4}, av[3] = {z4, z4, z4}, cq = z4, ck = z4, cv = z4;
  float s1[3] = {0.f, 0.f, 0.f}, s2[3] = {0.f, 0.f, 0.f};
  const bf16x8 ones = {0x3F80, 0x3F80, 0x3F80, 0x3F80, 0x3F80, 0x3F80, 0x3F80, 0x3F80};
  const unsigned o2 = ones2();
  const int r = lane & 15, g = lane >> 4;
  const unsigned xl = (2 * rh * 2 + (r >> 3)) * 1024 + (r & 7) * 128 + ((g ^ ((r >> 1) & 7)) << 4);
  const unsigned wl = wtr_lane(lane, hh);
#pragma unroll
  for (int c = 0; c < 8; ++c) {
    if (c == 0) WAITV(11);
    else if (c == 1 || c == 2) WAITV(8);
    else if (c == 7) WAITV(0);
    else WAITV(3);
    WAITL0();
    BAR();
    if (c >= 1 && c + 2 < 8) issue_w(c + 2);
#pragma unroll
    for (int s = 0; s < 2; ++s) {
      bf16x8 xf[3];
#pragma unroll
      for (int t = 0; t < 3; ++t) xf[t] = lds_frag(L + P1_X + c * 10240 + t * 2048 + (xl ^ (s << 6)));
      LAS unsigned char* wb = L + P1_W + (c % 3) * 24576 + s * 4096 + wl;
      const bf16x8 wq = tr_frag(wb), wk = tr_frag(wb + 8192), wv = tr_frag(wb + 16384);
      aq[0] = mfma32(wq, xf[1], aq[0]);
      aq[1] = mfma32(wq, xf[2], aq[1]);
#pragma unroll
      for (int t = 0; t < 3; ++t) ak[t] = mfma32(wk, xf[t], ak[t]);
#pragma unroll
      for (int t = 0; t < 3; ++t) av[t] = mfma32(xf[t], wv, av[t]);
      cq = mfma32(wq, ones, cq);
      ck = mfma32(wk, ones, ck);
      cv = mfma32(ones, wv, cv);
      if (((2 * c + s) & 3) == hh) {
#pragma unroll
        for (int t = 0; t < 3; ++t) {
          if (t == 0 && rh != 0) continue;
          const v4u u = __builtin_bit_cast(v4u, xf[t]);
#pragma unroll
          for (int j = 0; j < 4; ++j) { s1[t] = dot2(u[j], o2, s1[t]); s2[t] = dot2(u[j], u[j], s2[t]); }
        }
      }
    }
  }
  LAS f32x2* part = (LAS f32x2*)(L + P1_SM);
  LAS f32x2* fin = (LAS f32x2*)(L + P1_SM + 2560);
#pragma unroll
  for (int t = 0; t < 3; ++t) {
    if (t == 0 && rh != 0) continue;
    const float a = xsum_g(s1[t]), b = xsum_g(s2[t]);
    if (lane < 16) part[((2 * rh + t) * 4 + hh) * 16 + lane] = f32x2{a, b};
  }
  WAITL0();
  BAR();
  if (tid < 80) {
    const int T = tid >> 4, rr = tid & 15;
    float a = 0.f, b = 0.f;
#pragma unroll
    for (int h = 0; h < 4; ++h) { const f32x2 v = part[(T * 4 + h) * 16 + rr]; a += v[0]; b += v[1]; }
    const float mean = a * (1.f / D_);
    const float var = fmaxf(b * (1.f / D_) - mean * mean, 0.f);
    fin[tid] = f32x2{mean, __builtin_amdgcn_rsqf(var + LN_EPS)};
  }
  WAITL0();
  BAR();
  bf16x4 Qp[2], Kp[3], Vp[3];
#pragma unroll
  for (int t = 0; t < 3; ++t) {
    const f32x2 st = fin[(2 * rh + t) * 16 + r];
    v2u u;
    u[0] = pack2((ak[t][0] - st[0] * ck[0]) * st[1], (ak[t][1] - st[0] * ck[1]) * st[1]);
    u[1] = pack2((ak[t][2] - st[0] * ck[2]) * st[1], (ak[t][3] - st[0] * ck[3]) * st[1]);
    Kp[t] = __builtin_bit_cast(bf16x4, u);
    if (t >= 1) {
      u[0] = pack2((aq[t - 1][0] - st[0] * cq[0]) * st[1], (aq[t - 1][1] - st[0] * cq[1]) * st[1]);
      u[1] = pack2((aq[t - 1][2] - st[0] * cq[2]) * st[1], (aq[t - 1][3] - st[0] * cq[3]) * st[1]);
      Qp[t - 1] = __builtin_bit_cast(bf16x4, u);
    }
    const f32x4 s01 = *(LAS f32x4*)&fin[(2 * rh + t) * 16 + 4 * g], s23 = *(LAS f32x4*)&fin[(2 * rh + t) * 16 + 4 * g + 2];
    u[0] = pack2((av[t][0] - s01[0] * cv[0]) * s01[1], (av[t][1] - s01[2] * cv[0]) * s01[3]);
    u[1] = pack2((av[t][2] - s23[0] * cv[0]) * s23[1], (av[t][3] - s23[2] * cv[0]) * s23[3]);
    Vp[t] = __builtin_bit_cast(bf16x4, u);
  }

  v2u O[2];
#pragma unroll
  for (int qt = 0; qt < 2; ++qt) {
    const f32x4 S0 = mfma16(Kp[qt], Qp[qt], z4), S1 = mfma16(Kp[qt + 1], Qp[qt], z4);
    const bool prev_ok = !(rt == 0 && rh == 0 && qt == 0);
    float v[8];
    float m = -3.0e38f;
#pragma unroll
    for (int i = 0; i < 4; ++i) {
      const int d0 = 16 + r - (4 * g + i), d1 = r - (4 * g + i);
      const bool ok0 = prev_ok && d0 <= 14 && !(d0 & 1), ok1 = d1 >= 0 && d1 <= 14 && !(d1 & 1);
      v[i] = ok0 ? S0[i] : -3.0e38f;
      v[4 + i] = ok1 ? S1[i] : -3.0e38f;
      m = fmaxf(m, fmaxf(v[i], v[4 + i]));
    }
    m = xmax_g(m);
    float sum = 0.f;
    constexpr float SC = 0.25f * 1.4426950408889634f;
#pragma unroll
    for (int i = 0; i < 8; ++i) { v[i] = __builtin_amdgcn_exp2f((v[i] - m) * SC); sum += v[i]; }
    sum = xsum_g(sum);
    const float inv = __builtin_amdgcn_rcpf(sum);
    v2u p0, p1;
    p0[0] = pack2(v[0] * inv, v[1] * inv); p0[1] = pack2(v[2] * inv, v[3] * inv);
    p1[0] = pack2(v[4] * inv, v[5] * inv); p1[1] = pack2(v[6] * inv, v[7] * inv);
    f32x4 o = mfma16(Vp[qt], __builtin_bit_cast(bf16x4, p0), z4);
    o = mfma16(Vp[qt + 1], __builtin_bit_cast(bf16x4, p1), o);
    O[qt][0] = pack2(o[0], o[1]); O[qt][1] = pack2(o[2], o[3]);
  }
  {
    const auto a = __builtin_amdgcn_permlane16_swap(O[0][0], O[1][0], false, false);
    const auto b = __builtin_amdgcn_permlane16_swap(O[0][1], O[1][1], false, false);
    const v4u o = {a[0], b[0], a[1], b[1]};
    const int head = 4 * hg + hh;
    const unsigned off = WS_ATT + ((4 * rt + 2 * rh + (g & 1)) * 16 + (head >> 1)) * 1024 + ((2 * (head & 1) + (g >> 1)) * 16 + r) * 16;
    __builtin_amdgcn_raw_buffer_store_b128(o, rws, off, 0, AUX_SC1);
  }
}

DI void phase2(const Params& p, LAS unsigned char* L, int rt, int ct) {
  const int tid = threadIdx.x, lane = tid & 63, w = __builtin_amdgcn_readfirstlane(tid >> 6), mt = w & 3, kh = w >> 2;
  const __amdgpu_buffer_rsrc_t rw = mkrs(p.wo, D_ * D_ * 2), rws = mkrs(p.ws, WS_BYTES);
  const int r = lane & 15, g = lane >> 4;
  const unsigned wl0 = wdma_lane(lane, 1024, 0), wl1 = wdma_lane(lane, 1024, 1);
#pragma unroll
  for (int i = 0; i < 8; ++i) { const int P = w * 8 + i; dma16(rw, L + P * 1024, (P & 1) ? wl1 : wl0, P * 8 * 1024 + ct * 128); }
  bf16x8 a[8];
#pragma unroll
  for (int i = 0; i < 8; ++i)
    a[i] = __builtin_bit_cast(bf16x8, __builtin_amdgcn_raw_buffer_load_b128(rws, WS_ATT + ((4 * rt + mt) * 16 + kh * 8 + i) * 1024 + lane * 16, 0, AUX_SC1));
  const int row = 64 * rt + 16 * mt + r;
  v2u xr[2];
#pragma unroll
  for (int n = 0; n < 2; ++n) xr[n] = *(const GAS v2u*)(p.x + (size_t)row * D_ + 64 * ct + (2 * kh + n) * 16 + 4 * g);
  WAITV(0);
  BAR();
  const f32x4 z4 = {0.f, 0.f, 0.f, 0.f};
  f32x4 acc[4] = {z4, z4, z4, z4};
  unsigned wl[4];
#pragma unroll
  for (int n = 0; n < 4; ++n) wl[n] = wtr_lane(lane, n);
#pragma unroll
  for (int i = 0; i < 8; ++i)
#pragma unroll
    for (int n = 0; n < 4; ++n) acc[n] = mfma32(tr_frag(L + (kh * 8 + i) * 4096 + wl[n]), a[i], acc[n]);
  WAITL0();
  BAR();
  LAS f32x4* red = (LAS f32x4*)L;
#pragma unroll
  for (int n = 0; n < 2; ++n) red[((mt * 2 + kh) * 2 + n) * 64 + lane] = kh ? acc[n] : acc[2 + n];
  WAITL0();
  BAR();
  float h[2][4];
  float s1 = 0.f, s2 = 0.f;
  const unsigned o2 = ones2();
  v2u hp[2];
#pragma unroll
  for (int n = 0; n < 2; ++n) {
    const f32x4 o = red[((mt * 2 + (kh ^ 1)) * 2 + n) * 64 + lane];
    const f32x4 y = (kh ? acc[2 + n] : acc[n]) + o;
    const unsigned y01 = pack2(y[0], y[1]), y23 = pack2(y[2], y[3]);
    hp[n][0] = pack2(bflo(xr[n][0]) + bflo(y01), bfhi(xr[n][0]) + bfhi(y01));
    hp[n][1] = pack2(bflo(xr[n][1]) + bflo(y23), bfhi(xr[n][1]) + bfhi(y23));
    s1 = dot2(hp[n][0], o2, s1); s1 = dot2(hp[n][1], o2, s1);
    s2 = dot2(hp[n][0], hp[n][0], s2); s2 = dot2(hp[n][1], hp[n][1], s2);
  }
  s1 = xsum_g(s1); s2 = xsum_g(s2);
  {
    const auto a0 = __builtin_amdgcn_permlane16_swap(hp[0][0], hp[1][0], false, false);
    const auto a1 = __builtin_amdgcn_permlane16_swap(hp[0][1], hp[1][1], false, false);
    const v4u o = {a0[0], a1[0], a0[1], a1[1]};
    const int chunk = ((g & 1) << 1) | (g >> 1);
    __builtin_amdgcn_raw_buffer_store_b128(o, rws, WS_HF + ((4 * rt + mt) * 16 + 2 * ct + kh) * 1024 + (chunk * 16 + r) * 16, 0, AUX_SC1);
    if (lane < 16) {
      v2u sv = {__builtin_bit_cast(unsigned, s1), __builtin_bit_cast(unsigned, s2)};
      __builtin_amdgcn_raw_buffer_store_b64(sv, rws, WS_ST + (row * 16 + 2 * ct + kh) * 8, 0, AUX_SC1);
    }
  }
}

DI void phase3(const Params& p, LAS unsigned char* L, int pn, int fs) {
  const int tid = threadIdx.x, lane = tid & 63, w = __builtin_amdgcn_readfirstlane(tid >> 6);
  const __amdgpu_buffer_rsrc_t rg = mkrs(p.wg, D_ * F_ * 2), ru = mkrs(p.wu, D_ * F_ * 2), rws = mkrs(p.ws, WS_BYTES);
  const int r = lane & 15, g = lane >> 4;
  const unsigned wl0 = wdma_lane(lane, F_ * 2, 0), wl1 = wdma_lane(lane, F_ * 2, 1);
  bf16x8 wg[16], wu[16];
  const unsigned wl = wtr_lane(lane, w & 3) + (w >> 2) * 4096;
#pragma unroll
  for (int rd = 0; rd < 2; ++rd) {
#pragma unroll
    for (int i = 0; i < 16; ++i) {
      const int mat = i >> 3, cb = (i >> 2) & 1, pk = i & 3;
      dma16(mat ? ru : rg, L + (w * 16 + i) * 1024, (pk & 1) ? wl1 : wl0, (256 * rd + 32 * w + 8 * pk) * (F_ * 2) + fs * 256 + cb * 128);
    }
    WAITV(0);
    BAR();
#pragma unroll
    for (int ks = 0; ks < 8; ++ks) {
      wg[rd * 8 + ks] = tr_frag(L + ks * 16384 + wl);
      wu[rd * 8 + ks] = tr_frag(L + ks * 16384 + 8192 + wl);
    }
    WAITL0();
    BAR();
  }
  const f32x4 z4 = {0.f, 0.f, 0.f, 0.f};
  const bf16x8 ones = {0x3F80, 0x3F80, 0x3F80, 0x3F80, 0x3F80, 0x3F80, 0x3F80, 0x3F80};
  f32x4 cg = z4, cu = z4;
#pragma unroll
  for (int ks = 0; ks < 16; ++ks) { cg = mfma32(wg[ks], ones, cg); cu = mfma32(wu[ks], ones, cu); }
  LAS f32x2* fin = (LAS f32x2*)(L + 131072);
  {
    const int row = 128 * pn + (tid >> 2);
    const v4u a = __builtin_amdgcn_raw_buffer_load_b128(rws, WS_ST + (row * 16 + 4 * (tid & 3)) * 8, 0, AUX_SC1);
    const v4u b = __builtin_amdgcn_raw_buffer_load_b128(rws, WS_ST + (row * 16 + 4 * (tid & 3)) * 8 + 16, 0, AUX_SC1);
    float s1 = asf(a[0]) + asf(a[2]) + asf(b[0]) + asf(b[2]);
    float s2 = asf(a[1]) + asf(a[3]) + asf(b[1]) + asf(b[3]);
    s1 += __shfl_xor(s1, 1); s2 += __shfl_xor(s2, 1);
    s1 += __shfl_xor(s1, 2); s2 += __shfl_xor(s2, 2);
    const float mean = s1 * (1.f / D_);
    const float var = fmaxf(s2 * (1.f / D_) - mean * mean, 0.f);
    if ((tid & 3) == 0) fin[tid >> 2] = f32x2{mean, __builtin_amdgcn_rsqf(var + LN_EPS)};
  }
#pragma unroll
  for (int c = 0; c < 4; ++c) {
    v4u st[4];
#pragma unroll
    for (int i = 0; i < 4; ++i) st[i] = __builtin_amdgcn_raw_buffer_load_b128(rws, WS_HF + ((8 * pn + w) * 16 + 4 * c + i) * 1024 + lane * 16, 0, AUX_SC1);
#pragma unroll
    for (int i = 0; i < 4; ++i) *(LAS v4u*)(L + (w * 16 + 4 * c + i) * 1024 + lane * 16) = st[i];
  }
  WAITL0();
  BAR();
  f32x4 ag[8], au[8];
#pragma unroll
  for (int m = 0; m < 8; ++m) {
    const float nm = -fin[m * 16 + r][0];
    ag[m] = cg * nm;
    au[m] = cu * nm;
  }
#pragma unroll
  for (int ks = 0; ks < 16; ++ks)
#pragma unroll
    for (int m = 0; m < 8; ++m) {
      const bf16x8 a = lds_frag(L + (m * 16 + ks) * 1024 + lane * 16);
      ag[m] = mfma32(wg[ks], a, ag[m]);
      au[m] = mfma32(wu[ks], a, au[m]);
    }
  constexpr float K1 = -2.302208198f, K2 = -0.1029432397f;
#pragma unroll
  for (int m = 0; m < 8; m += 2) {
    v2u o[2];
#pragma unroll
    for (int mm = 0; mm < 2; ++mm) {
      const float rs = fin[(m + mm) * 16 + r][1];
      float a[4];
#pragma unroll
      for (int i = 0; i < 4; ++i) {
        const float gg = ag[m + mm][i] * rs;
        const float e = __builtin_amdgcn_exp2f(gg * fmaf(gg * gg, K2, K1));
        a[i] = gg * rs * au[m + mm][i] * __builtin_amdgcn_rcpf(1.f + e);
      }
      o[mm][0] = pack2(a[0], a[1]); o[mm][1] = pack2(a[2], a[3]);
    }
    const auto a0 = __builtin_amdgcn_permlane16_swap(o[0][0], o[1][0], false, false);
    const auto a1 = __builtin_amdgcn_permlane16_swap(o[0][1], o[1][1], false, false);
    const v4u ov = {a0[0], a1[0], a0[1], a1[1]};
    __builtin_amdgcn_raw_buffer_store_b128(ov, rws, WS_ACT + ((8 * pn + m + (g & 1)) * 56 + 4 * fs + (w >> 1)) * 1024 + ((2 * (w & 1) + (g >> 1)) * 16 + r) * 16, 0, AUX_SC1);
  }
}

DI void phase4(const Params& p, LAS unsigned char* L, int rt, int ct) {
  const int tid = threadIdx.x, lane = tid & 63, w = __builtin_amdgcn_readfirstlane(tid >> 6), mt = w & 3, kh = w >> 2;
  const __amdgpu_buffer_rsrc_t rw = mkrs(p.wd, F_ * D_ * 2), rws = mkrs(p.ws, WS_BYTES);
  const int r = lane & 15, g = lane >> 4;
  const unsigned wl0 = wdma_lane(lane, 1024, 0), wl1 = wdma_lane(lane, 1024, 1);
  auto issue_w = [&](int c) {
#pragma unroll
    for (int j = 0; j < 2; ++j) {
      const int i = w * 2 + j, half = i >> 3, ksl = (i >> 2) & 1, pk = i & 3;
      dma16(rw, L + (c % 9) * 16384 + i * 1024, (pk & 1) ? wl1 : wl0, ((half * 28 + 2 * c + ksl) * 32 + 8 * pk) * 1024 + ct * 128);
    }
  };
#pragma unroll
  for (int c = 0; c < 9; ++c) issue_w(c);
  bf16x8 a[28];
#pragma unroll
  for (int i = 0; i < 28; ++i)
    a[i] = __builtin_bit_cast(bf16x8, __builtin_amdgcn_raw_buffer_load_b128(rws, WS_ACT + ((4 * rt + mt) * 56 + kh * 28 + i) * 1024 + lane * 16, 0, AUX_SC1));
  const v4u hr = __builtin_amdgcn_raw_buffer_load_b128(rws, WS_HF + ((4 * rt + mt) * 16 + 2 * ct + kh) * 1024 + ((((g & 1) << 1) | (g >> 1)) * 16 + r) * 16, 0, AUX_SC1);
  WAITV(0);
  BAR();
  const f32x4 z4 = {0.f, 0.f, 0.f, 0.f};
  f32x4 acc[4] = {z4, z4, z4, z4};
  unsigned wl[4];
#pragma unroll
  for (int n = 0; n < 4; ++n) wl[n] = wtr_lane(lane, n) + kh * 8192;
#pragma unroll
  for (int c = 0; c < 14; ++c) {
    if (c == 9) { WAITV(0); BAR(); }
#pragma unroll
    for (int s = 0; s < 2; ++s)
#pragma unroll
      for (int n = 0; n < 4; ++n) acc[n] = mfma32(tr_frag(L + (c % 9) * 16384 + s * 4096 + wl[n]), a[2 * c + s], acc[n]);
    if (c == 4) { WAITL0(); BAR(); issue_w(9); issue_w(10); issue_w(11); issue_w(12); issue_w(13); }
  }
  WAITL0();
  BAR();
  LAS f32x4* red = (LAS f32x4*)L;
#pragma unroll
  for (int n = 0; n < 2; ++n) red[((mt * 2 + kh) * 2 + n) * 64 + lane] = kh ? acc[n] : acc[2 + n];
  WAITL0();
  BAR();
  const auto b0 = __builtin_amdgcn_permlane16_swap(hr[0], hr[2], false, false);
  const auto b1 = __builtin_amdgcn_permlane16_swap(hr[1], hr[3], false, false);
  v2u op[2];
#pragma unroll
  for (int n = 0; n < 2; ++n) {
    const f32x4 o = red[((mt * 2 + (kh ^ 1)) * 2 + n) * 64 + lane];
    const f32x4 y = (kh ? acc[2 + n] : acc[n]) + o;
    const unsigned y01 = pack2(y[0], y[1]), y23 = pack2(y[2], y[3]);
    op[n][0] = pack2(bflo(b0[n]) + bflo(y01), bfhi(b0[n]) + bfhi(y01));
    op[n][1] = pack2(bflo(b1[n]) + bflo(y23), bfhi(b1[n]) + bfhi(y23));
  }
  const auto a0 = __builtin_amdgcn_permlane16_swap(op[0][0], op[1][0], false, false);
  const auto a1 = __builtin_amdgcn_permlane16_swap(op[0][1], op[1][1], false, false);
  const v4u o = {a0[0], a1[0], a0[1], a1[1]};
  const int chunk = ((g & 1) << 1) | (g >> 1);
  *(GAS v4u*)(p.out + (size_t)(64 * rt + 16 * mt + r) * D_ + 64 * ct + 32 * kh + chunk * 8) = o;
}

__global__ __launch_bounds__(512, 2) void k_p1(Params p) {
  extern __shared__ __attribute__((aligned(16))) unsigned char smem[];
  const int b = blockIdx.x, xcd = b & 7, i = b >> 3;
  const int rt = (xcd >> 1) * 8 + (i >> 2), hg = (xcd & 1) * 4 + (i & 3);
  phase1(p, (LAS unsigned char*)smem, rt, hg, (rt + hg) & 7);
}
__global__ __launch_bounds__(512, 2) void k_p2(Params p) {
  extern __shared__ __attribute__((aligned(16))) unsigned char smem[];
  const int b = blockIdx.x, xcd = b & 7, i = b >> 3;
  phase2(p, (LAS unsigned char*)smem, xcd * 4 + (i >> 3), i & 7);
}
__global__ __launch_bounds__(512, 2) void k_p3(Params p) {
  extern __shared__ __attribute__((aligned(16))) unsigned char smem[];
  const int b = blockIdx.x, xcd = b & 7, i = b >> 3;
  if (i >= 28) return;
  phase3(p, (LAS unsigned char*)smem, (xcd >> 1) * 4 + i / 7, (xcd & 1) * 7 + i % 7);
}
__global__ __launch_bounds__(512, 2) void k_p4(Params p) {
  extern __shared__ __attribute__((aligned(16))) unsigned char smem[];
  const int b = blockIdx.x, xcd = b & 7, i = b >> 3;
  phase4(p, (LAS unsigned char*)smem, xcd * 4 + (i >> 3), i & 7);
}

extern "C" void kernel_launch(void* const* d_in, const int* in_sizes, int n_in, void* d_out, int out_size, void* d_ws, size_t ws_size, hipStream_t stream) {
  Params p;
  p.x = (const unsigned short*)d_in[0];
  p.wqkv = (const unsigned short*)d_in[1];
  p.wo = (const unsigned short*)d_in[2];
  p.wg = (const unsigned short*)d_in[4];
  p.wu = (const unsigned short*)d_in[5];
  p.wd = (const unsigned short*)d_in[6];
  p.out = (unsigned short*)d_out;
  p.ws = (unsigned char*)d_ws;
  constexpr int LDS = 160 * 1024;
  {
    (void)hipFuncSetAttribute((const void*)k_p1, hipFuncAttributeMaxDynamicSharedMemorySize, LDS);
    (void)hipFuncSetAttribute((const void*)k_p2, hipFuncAttributeMaxDynamicSharedMemorySize, LDS);
    (void)hipFuncSetAttribute((const void*)k_p3, hipFuncAttributeMaxDynamicSharedMemorySize, LDS);
    (void)hipFuncSetAttribute((const void*)k_p4, hipFuncAttributeMaxDynamicSharedMemorySize, LDS);
  }
  k_p1<<<256, 512, LDS, stream>>>(p);
  k_p2<<<256, 512, LDS, stream>>>(p);
  k_p3<<<256, 512, LDS, stream>>>(p);
  k_p4<<<256, 512, LDS, stream>>>(p);
}
```

```cpp
#include <hip/hip_runtime.h>
#include <hip/hip_bf16.h>

#define GAS __attribute__((address_space(1)))
#define LAS __attribute__((address_space(3)))
typedef short bf16x8 __attribute__((ext_vector_type(8)));
typedef short bf16x4 __attribute__((ext_vector_type(4)));
typedef __bf16 bf2 __attribute__((ext_vector_type(2)));
typedef float f32x4 __attribute__((ext_vector_type(4)));
typedef float f32x2 __attribute__((ext_vector_type(2)));
typedef float f32x16 __attribute__((ext_vector_type(16)));
typedef unsigned v4u __attribute__((ext_vector_type(4)));
typedef unsigned v2u __attribute__((ext_vector_type(2)));
#define DI __device__ __forceinline__

constexpr int S_ = 2048, D_ = 512, F_ = 1792;
constexpr int AUX_SC1 = 16;
constexpr unsigned RSRC3 = 0x00020000u;
constexpr unsigned WS_ATT = 0u;
constexpr unsigned WS_HF = 2u << 20;
constexpr unsigned WS_ACT = 4u << 20;
constexpr unsigned WS_ST = 12u << 20;
constexpr unsigned WS_FL = 13u << 20;
constexpr unsigned FL_E = 0, FL_F1 = 4096, FL_C2 = 16384, FL_C3 = 24576, FL_TMO = 28672;
constexpr unsigned WS_BYTES = 14u << 20;
typedef GAS unsigned gu32;
#define RLX_AGENT __ATOMIC_RELAXED, __HIP_MEMORY_SCOPE_AGENT
constexpr unsigned SPIN_CAP = 1u << 18;

struct Params {
  const unsigned short* x;
  const unsigned short* wqkv;
  const unsigned short* wo;
  const unsigned short* wg;
  const unsigned short* wu;
  const unsigned short* wd;
  unsigned short* out;
  unsigned char* ws;
};

#if defined(__HIP_DEVICE_COMPILE__)
#pragma clang attribute push (__attribute__((target("no-packed-fp32-ops"))), apply_to = function)
#endif
template <int V> struct IC { static constexpr int value = V; };
template <int A, int B, class F> __device__ __forceinline__ void static_for(F&& f) { if constexpr (A < B) { f(IC<A>{}); static_for<A + 1, B>(f); } }
DI float asf(unsigned u) { return __builtin_bit_cast(float, u); }
DI unsigned asu(float f) { return __builtin_bit_cast(unsigned, f); }
DI f32x4 mfma32(bf16x8 a, bf16x8 b, f32x4 c) { return __builtin_amdgcn_mfma_f32_16x16x32_bf16(a, b, c, 0, 0, 0); }
DI f32x4 mfma16(bf16x4 a, bf16x4 b, f32x4 c) { return __builtin_amdgcn_mfma_f32_16x16x16bf16_1k(a, b, c, 0, 0, 0); }
DI unsigned pack2(float lo, float hi) { bf2 p; p[0] = (__bf16)lo; p[1] = (__bf16)hi; return __builtin_bit_cast(unsigned, p); }
DI float bflo(unsigned u) { return __builtin_bit_cast(float, u << 16); }
DI float bfhi(unsigned u) { return __builtin_bit_cast(float, u & 0xffff0000u); }
DI unsigned ones2() { unsigned o = 0x3F803F80u; asm volatile("" : "+v"(o)); return o; }
DI float dot2(unsigned a, unsigned b, float c) { return __builtin_amdgcn_fdot2_f32_bf16(__builtin_bit_cast(bf2, a), __builtin_bit_cast(bf2, b), c, false); }
DI __amdgpu_buffer_rsrc_t mkrs(const void* p, unsigned bytes) { return __builtin_amdgcn_make_buffer_rsrc((void*)p, 0, bytes, RSRC3); }
DI void dma16(__amdgpu_buffer_rsrc_t rs, unsigned dst, unsigned voff, unsigned soff) {
  asm volatile("s_mov_b32 m0, %0\n\ts_nop 0\n\tbuffer_load_dwordx4 %1, %2, %3 offen lds"
               :: "s"(dst), "v"(voff), "s"(rs), "s"(soff) : "m0", "memory");
}
template <int SS> DI void dma16x4(__amdgpu_buffer_rsrc_t rs, unsigned dst, unsigned v0, unsigned v1, unsigned soff) {
  unsigned t;
  asm volatile("s_mov_b32 m0, %1\n\ts_add_u32 %0, %5, %6\n\t"
               "buffer_load_dwordx4 %2, %4, %5 offen lds\n\t"
               "buffer_load_dwordx4 %3, %4, %0 offen offset:1024 lds\n\ts_add_u32 %0, %0, %6\n\t"
               "buffer_load_dwordx4 %2, %4, %0 offen offset:2048 lds\n\ts_add_u32 %0, %0, %6\n\t"
               "buffer_load_dwordx4 %3, %4, %0 offen offset:3072 lds"
               : "=&s"(t) : "s"(dst), "v"(v0), "v"(v1), "s"(rs), "s"(soff), "n"(SS - 1024) : "m0", "scc", "memory");
}
template <int LS> DI void dma16x3(__amdgpu_buffer_rsrc_t rs, unsigned dst, unsigned v, unsigned soff) {
  asm volatile("s_mov_b32 m0, %0\n\ts_nop 0\n\t"
               "buffer_load_dwordx4 %1, %2, %3 offen lds\n\ts_add_u32 m0, m0, %4\n\ts_nop 0\n\t"
               "buffer_load_dwordx4 %1, %2, %3 offen offset:1024 lds\n\ts_add_u32 m0, m0, %4\n\ts_nop 0\n\t"
               "buffer_load_dwordx4 %1, %2, %3 offen offset:2048 lds"
               :: "s"(dst), "v"(v), "s"(rs), "s"(soff), "n"(LS - 1024) : "m0", "scc", "memory");
}
DI bf16x8 lds_frag(LAS unsigned char* p) { return *(LAS bf16x8*)p; }
DI bf16x8 tr_frag(LAS unsigned char* p) {
  bf16x4 lo = __builtin_amdgcn_ds_read_tr16_b64_v4i16((LAS bf16x4*)p);
  bf16x4 hi = __builtin_amdgcn_ds_read_tr16_b64_v4i16((LAS bf16x4*)(p + 512));
  return __builtin_shufflevector(lo, hi, 0, 1, 2, 3, 4, 5, 6, 7);
}
DI void swap16(float v, float& a, float& b) {
  const unsigned u = asu(v);
  auto r = __builtin_amdgcn_permlane16_swap(u, u, false, false);
  a = asf(r[0]); b = asf(r[1]);
}
DI void swap32(float v, float& a, float& b) {
  const unsigned u = asu(v);
  auto r = __builtin_amdgcn_permlane32_swap(u, u, false, false);
  a = asf(r[0]); b = asf(r[1]);
}
DI float xsum_g(float v) {
  const f32x4 z = {0.f, 0.f, 0.f, 0.f};
  asm volatile("s_nop 3" : "+v"(v));
  return __builtin_amdgcn_mfma_f32_16x16x4f32(1.0f, v, z, 0, 0, 0)[0];
}
DI float xmax_g(float v) {
  float a, b;
  swap16(v, a, b); v = fmaxf(a, b);
  swap32(v, a, b); return fmaxf(a, b);
}
#define WAITV(n) asm volatile("s_waitcnt vmcnt(" #n ")" ::: "memory")
#define WAITL0() asm volatile("s_waitcnt lgkmcnt(0)" ::: "memory")
#define BAR() asm volatile("s_barrier" ::: "memory")
#define SB() __builtin_amdgcn_sched_barrier(0)
#define SG(mask, n) __builtin_amdgcn_sched_group_barrier(mask, n, 0)
#define M_MFMA 0x8
#define M_DSR 0x100
#define M_DSW 0x200
#define M_VALU 0x2
#define M_TRANS 0x400
#define M_VMEMR 0x20
constexpr float LN_EPS = 1e-5f;
#define STAMP_DECL do {} while (0)
#define STAMP(i) do {} while (0)
#define STAMP_END() do {} while (0)

DI unsigned wtr_lane(int lane, int nt) {
  const int g = lane >> 4, q = (lane & 15) >> 2, p = lane & 3;
  const int sg = (q >> 1) | ((g & 1) << 1);
  return g * 1024 + q * 128 + ((nt ^ sg) << 5) + p * 8;
}
DI unsigned wdma_lane(int lane, unsigned pitch, int odd) {
  const int r8 = lane >> 3, pos = lane & 7;
  return r8 * pitch + ((pos ^ (((r8 >> 1) & 1) << 1) ^ (odd << 2)) << 4);
}

constexpr int P1_X = 0;
constexpr int P1_W = 81920;
constexpr int SM = 155648;
constexpr int P1_PART = SM;
constexpr int P1_CS = SM + 2560;
constexpr int P1_FIN = SM + 4096;
constexpr int P3_CS = SM;
constexpr int WO_IMG = 65536;
constexpr int RED = 131072;
constexpr int GU_A = 0;
constexpr int GU_B = 65536;
DI constexpr int wd_slot(int c) { return (c % 9) == 0 ? 131072 : ((c % 9) - 1) * 16384; }

DI bool poll_fail(unsigned& spins, gu32* fl, int lane) {
  __builtin_amdgcn_s_sleep(1);
  if (++spins > SPIN_CAP) { if (lane == 0) __hip_atomic_store(fl + FL_TMO / 4, 1u, RLX_AGENT); return true; }
  return false;
}

__global__ __launch_bounds__(512, 2) void k_fused(Params p) {
  extern __shared__ __attribute__((aligned(16))) unsigned char smem[];
  LAS unsigned char* L = (LAS unsigned char*)smem;
  const int tid = threadIdx.x, lane = tid & 63, w = __builtin_amdgcn_readfirstlane(tid >> 6);
  const int bx = blockIdx.x & 7, bi = blockIdx.x >> 3, rt = 4 * bx + (bi >> 3), c8 = bi & 7;
  const int pr = bx >> 1, pc = bx & 1;
  const int r = lane & 15, g = lane >> 4;
  const __amdgpu_buffer_rsrc_t rws = mkrs(p.ws, WS_BYTES);
  gu32* fl = (gu32*)(p.ws + WS_FL);
  const f32x4 z4 = {0.f, 0.f, 0.f, 0.f};
  const bf16x8 ones = {0x3F80, 0x3F80, 0x3F80, 0x3F80, 0x3F80, 0x3F80, 0x3F80, 0x3F80};
  const unsigned o2 = ones2();
  const int rt1 = 8 * pr + (bi >> 2), hg = 4 * pc + (bi & 3);
  const bool has3 = bi < 28;
  const int pn = 2 * pr + (bi >= 14 ? 1 : 0), fs = 14 * pc + (bi >= 14 ? bi - 14 : bi);
  STAMP_DECL;
  STAMP(0);
  unsigned tagv = 0;
  if (w == 0) {
    tagv = __hip_atomic_load(fl + (FL_E + (2 * pr + (lane & 1)) * 256) / 4, RLX_AGENT) + 1u;
    if (hg == 0 && lane == 0) {
      __hip_atomic_store(fl + (FL_C2 + rt1 * 128) / 4, 0u, RLX_AGENT);
      if ((rt1 & 3) == 0) __hip_atomic_store(fl + (FL_C3 + (rt1 >> 2) * 128) / 4, 0u, RLX_AGENT);
    }
  }
  {
    const int hh = w & 3, rh = w >> 2, rot = (bi + (bi >> 2) + 2 * pr) & 7;
    const __amdgpu_buffer_rsrc_t rx = mkrs(p.x, S_ * D_ * 2), rw = mkrs(p.wqkv, D_ * 3 * D_ * 2);
    const int r8 = lane >> 3, pos = lane & 7;
    const unsigned xv0 = r8 * 1024 + ((pos ^ (r8 >> 1)) << 4), xv1 = xv0 ^ 64;
    auto issue_x = [&](int half) {
      const int c = 4 * half + (w >> 1);
#pragma unroll
      for (int i = 0; i < 5; ++i) {
        const int P = 5 * (w & 1) + i;
        int row = 64 * rt1 - 16 + 8 * P;
        row = row < 0 ? 0 : row;
        dma16(rx, P1_X + (c * 10 + P) * 1024, (P & 1) ? xv1 : xv0, row * 1024 + ((c + rot) & 7) * 128);
      }
    };
    const unsigned wv = wdma_lane(lane, 3072, w & 1);
    auto issue_w = [&](int c) {
      dma16x3<8192>(rw, P1_W + (c % 3) * 24576 + w * 1024, wv, (((c + rot) & 7) * 64 + 8 * w) * 3072 + hg * 128);
    };
    issue_x(0); issue_w(0); issue_w(1); issue_w(2);
    STAMP(1);
    f32x4 aq[2] = {z4, z4}, ak[3] = {z4, z4, z4}, av[3] = {z4, z4, z4}, cq = z4, ck = z4, cv = z4;
    float s1[3] = {0.f, 0.f, 0.f}, s2[3] = {0.f, 0.f, 0.f};
    const unsigned xl = (2 * rh * 2 + (r >> 3)) * 1024 + (r & 7) * 128 + ((g ^ ((r >> 1) & 7)) << 4);
    const unsigned wl = wtr_lane(lane, hh);
    const int sq = (hh + 2 * rh) & 3;
    struct F1 { bf16x8 x[3], q, k, v; };
    auto ld1 = [&](F1& f, int c, int s) {
      LAS unsigned char* wb = L + P1_W + (c % 3) * 24576 + s * 4096 + wl;
      f.q = tr_frag(wb);
      f.x[1] = lds_frag(L + P1_X + c * 10240 + 1 * 2048 + (xl ^ (s << 6)));
      f.x[2] = lds_frag(L + P1_X + c * 10240 + 2 * 2048 + (xl ^ (s << 6)));
      f.k = tr_frag(wb + 8192);
      f.x[0] = lds_frag(L + P1_X + c * 10240 + 0 * 2048 + (xl ^ (s << 6)));
      f.v = tr_frag(wb + 16384);
    };
    auto mm1 = [&](const F1& f) {
      aq[0] = mfma32(f.q, f.x[1], aq[0]);
      aq[1] = mfma32(f.q, f.x[2], aq[1]);
      ak[1] = mfma32(f.k, f.x[1], ak[1]);
      ak[2] = mfma32(f.k, f.x[2], ak[2]);
      ak[0] = mfma32(f.k, f.x[0], ak[0]);
      av[0] = mfma32(f.x[0], f.v, av[0]);
      av[1] = mfma32(f.x[1], f.v, av[1]);
      av[2] = mfma32(f.x[2], f.v, av[2]);
    };
    auto ex1 = [&](const F1& f, int ks) {
      if ((ks & 1) == rh) {
        cq = mfma32(f.q, ones, cq);
        ck = mfma32(f.k, ones, ck);
        cv = mfma32(ones, f.v, cv);
      }
      if ((ks & 3) == sq) {
#pragma unroll
        for (int t = 0; t < 3; ++t) {
          if (t == 0 && rh != 0) continue;
          const v4u u = __builtin_bit_cast(v4u, f.x[t]);
#pragma unroll
          for (int j = 0; j < 4; ++j) { s1[t] = dot2(u[j], o2, s1[t]); s2[t] = dot2(u[j], u[j], s2[t]); }
        }
      }
    };
#define ILV1() do { _Pragma("unroll") for (int _i = 0; _i < 8; ++_i) { SG(M_MFMA, 1); SG(M_DSR, 1); } SG(M_DSR, 1); } while (0)
    F1 f0, f1;
    WAITV(6);
    BAR();
    STAMP(2);
    ld1(f0, 0, 0);
    SB();
#pragma unroll
    for (int c = 0; c < 8; ++c) {
      ld1(f1, c, 1);
      mm1(f0);
      ILV1();
      SB();
      ex1(f0, 2 * c);
      SB();
      if (c < 7) {
        if (c == 1 || c == 2) WAITV(8);
        else if (c == 6) WAITV(0);
        else WAITV(3);
        WAITL0();
        BAR();
        STAMP(3 + c);
        if (c + 3 < 8) issue_w(c + 3);
        if (c == 0) issue_x(1);
        SB();
        ld1(f0, c + 1, 0);
        mm1(f1);
        ILV1();
      } else {
        mm1(f1);
      }
      SB();
      ex1(f1, 2 * c + 1);
      SB();
    }
    STAMP(10);
    LAS f32x2* part = (LAS f32x2*)(L + P1_PART);
    LAS float* csx = (LAS float*)(L + P1_CS);
#pragma unroll
    for (int t = 0; t < 3; ++t) {
      if (t == 0 && rh != 0) continue;
      const float a = xsum_g(s1[t]), b = xsum_g(s2[t]);
      if (lane < 16) part[((2 * rh + t) * 16 + lane) * 4 + hh] = f32x2{a, b};
    }
    if (r == 0) { *(LAS f32x4*)&csx[w * 48 + 4 * g] = cq; *(LAS f32x4*)&csx[w * 48 + 16 + 4 * g] = ck; }
    if (lane < 16) csx[w * 48 + 32 + lane] = cv[0];
    WAITL0();
    BAR();
    {
      const __amdgpu_buffer_rsrc_t rwo = mkrs(p.wo, D_ * D_ * 2);
      const unsigned a0 = wdma_lane(lane, 1024, 0), a1 = a0 ^ 64;
      dma16x4<8192>(rwo, WO_IMG + w * 8192, a0, a1, w * 65536 + c8 * 128);
      dma16x4<8192>(rwo, WO_IMG + w * 8192 + 4096, a0, a1, w * 65536 + 32768 + c8 * 128);
    }
    {
      const f32x4 oq = *(LAS f32x4*)&csx[(w ^ 4) * 48 + 4 * g], ok = *(LAS f32x4*)&csx[(w ^ 4) * 48 + 16 + 4 * g];
      const float ov = csx[(w ^ 4) * 48 + 32 + r];
      cq += oq; ck += ok; cv[0] += ov;
    }
    float mean[3], rstd[3];
    LAS f32x2* fin = (LAS f32x2*)(L + P1_FIN) + w * 48;
#pragma unroll
    for (int t = 0; t < 3; ++t) {
      const f32x4 a = *(LAS f32x4*)&part[((2 * rh + t) * 16 + r) * 4], b = *(LAS f32x4*)&part[((2 * rh + t) * 16 + r) * 4 + 2];
      const float sa = (a[0] + a[2]) + (b[0] + b[2]), sb = (a[1] + a[3]) + (b[1] + b[3]);
      mean[t] = sa * (1.f / D_);
      rstd[t] = __builtin_amdgcn_rsqf(fmaxf(sb * (1.f / D_) - mean[t] * mean[t], 0.f) + LN_EPS);
      fin[t * 16 + r] = f32x2{mean[t], rstd[t]};
    }
    WAITL0();
    STAMP(11);
    bf16x4 Qp[2], Kp[3], Vp[3];
#pragma unroll
    for (int t = 0; t < 3; ++t) {
      v2u u;
      u[0] = pack2((ak[t][0] - mean[t] * ck[0]) * rstd[t], (ak[t][1] - mean[t] * ck[1]) * rstd[t]);
      u[1] = pack2((ak[t][2] - mean[t] * ck[2]) * rstd[t], (ak[t][3] - mean[t] * ck[3]) * rstd[t]);
      Kp[t] = __builtin_bit_cast(bf16x4, u);
      if (t >= 1) {
        u[0] = pack2((aq[t - 1][0] - mean[t] * cq[0]) * rstd[t], (aq[t - 1][1] - mean[t] * cq[1]) * rstd[t]);
        u[1] = pack2((aq[t - 1][2] - mean[t] * cq[2]) * rstd[t], (aq[t - 1][3] - mean[t] * cq[3]) * rstd[t]);
        Qp[t - 1] = __builtin_bit_cast(bf16x4, u);
      }
      const f32x4 s01 = *(LAS f32x4*)&fin[t * 16 + 4 * g], s23 = *(LAS f32x4*)&fin[t * 16 + 4 * g + 2];
      u[0] = pack2((av[t][0] - s01[0] * cv[0]) * s01[1], (av[t][1] - s01[2] * cv[0]) * s01[3]);
      u[1] = pack2((av[t][2] - s23[0] * cv[0]) * s23[1], (av[t][3] - s23[2] * cv[0]) * s23[3]);
      Vp[t] = __builtin_bit_cast(bf16x4, u);
    }
    f32x4 b0, b1;
#pragma unroll
    for (int i = 0; i < 4; ++i) {
      const int d0 = 16 + r - (4 * g + i), d1 = r - (4 * g + i);
      b0[i] = (d0 <= 14 && !(d0 & 1)) ? 0.f : -1.0e30f;
      b1[i] = (d1 >= 0 && d1 <= 14 && !(d1 & 1)) ? 0.f : -1.0e30f;
    }
    v2u O[2];
#pragma unroll
    for (int qt = 0; qt < 2; ++qt) {
      f32x4 bb = b0;
      if (qt == 0 && rt1 == 0 && rh == 0) bb = f32x4{-1.0e30f, -1.0e30f, -1.0e30f, -1.0e30f};
      const f32x4 S0 = mfma16(Kp[qt], Qp[qt], bb), S1 = mfma16(Kp[qt + 1], Qp[qt], b1);
      float m = fmaxf(fmaxf(fmaxf(S0[0], S0[1]), fmaxf(S0[2], S0[3])), fmaxf(fmaxf(S1[0], S1[1]), fmaxf(S1[2], S1[3])));
      m = xmax_g(m);
      constexpr float SC = 0.25f * 1.4426950408889634f;
      const float ms = -m * SC;
      float v[8];
#pragma unroll
      for (int i = 0; i < 4; ++i) { v[i] = __builtin_amdgcn_exp2f(fmaf(S0[i], SC, ms)); v[4 + i] = __builtin_amdgcn_exp2f(fmaf(S1[i], SC, ms)); }
      float sum = ((v[0] + v[1]) + (v[2] + v[3])) + ((v[4] + v[5]) + (v[6] + v[7]));
      sum = xsum_g(sum);
      const float inv = __builtin_amdgcn_rcpf(sum);
      v2u p0, p1;
      p0[0] = pack2(v[0] * inv, v[1] * inv); p0[1] = pack2(v[2] * inv, v[3] * inv);
      p1[0] = pack2(v[4] * inv, v[5] * inv); p1[1] = pack2(v[6] * inv, v[7] * inv);
      f32x4 o = mfma16(Vp[qt], __builtin_bit_cast(bf16x4, p0), z4);
      o = mfma16(Vp[qt + 1], __builtin_bit_cast(bf16x4, p1), o);
      O[qt][0] = pack2(o[0], o[1]); O[qt][1] = pack2(o[2], o[3]);
    }
    {
      const auto a = __builtin_amdgcn_permlane16_swap(O[0][0], O[1][0], false, false);
      const auto b = __builtin_amdgcn_permlane16_swap(O[0][1], O[1][1], false, false);
      const v4u o = {a[0], b[0], a[1], b[1]};
      const int head = 4 * hg + hh;
      const unsigned off = WS_ATT + ((4 * rt1 + 2 * rh + (g & 1)) * 16 + (head >> 1)) * 1024 + ((2 * (head & 1) + (g >> 1)) * 16 + r) * 16;
      __builtin_amdgcn_raw_buffer_store_b128(o, rws, off, 0, AUX_SC1);
    }
    STAMP(12);
    WAITV(0);
    BAR();
    if (tid == ((rt1 >> 2) & 1)) __hip_atomic_store(fl + (FL_F1 + rt1 * 128) / 4 + hg, tagv, RLX_AGENT);
    STAMP(13);
  }
  const __amdgpu_buffer_rsrc_t rg = mkrs(p.wg, D_ * F_ * 2), ru = mkrs(p.wu, D_ * F_ * 2);
  const unsigned gl0 = wdma_lane(lane, F_ * 2, 0), gl1 = gl0 ^ 64;
  auto issue_gu = [&](int half, int dst) {
    const unsigned so = ((8 * half + w) * 32) * (F_ * 2) + fs * 128;
    dma16x4<8 * F_ * 2>(rg, dst + w * 8192, gl0, gl1, so);
    dma16x4<8 * F_ * 2>(ru, dst + w * 8192 + 4096, gl1, gl0, so);
  };
  if (has3 && w != 0) issue_gu(0, GU_A);
  v2u hp[2];
  {
    const int mt = w & 3, kh = w >> 2, ct = c8;
    const int row = 64 * rt + 16 * mt + r;
    bf16x8 wf[8][4];
    {
      unsigned wl[4];
#pragma unroll
      for (int n = 0; n < 4; ++n) wl[n] = wtr_lane(lane, n) + WO_IMG + kh * 32768;
#pragma unroll
      for (int i = 0; i < 8; ++i)
#pragma unroll
        for (int n = 0; n < 4; ++n) wf[i][n] = tr_frag(L + i * 4096 + wl[n]);
    }
    if (w == 0) {
      const int frt = lane < 8 ? rt : lane < 12 ? 4 * bx + (lane & 3) : 4 * pn + (lane & 3);
      const unsigned idx = (FL_F1 + frt * 128) / 4 + (lane < 8 ? lane : 0);
      const unsigned want = __shfl(tagv, (frt >> 2) & 1);
      for (unsigned spins = 0;;) {
        const bool ok = lane >= 16 || __hip_atomic_load(fl + idx, RLX_AGENT) == want;
        if (__all(ok)) break;
        if (poll_fail(spins, fl, lane)) break;
      }
      __builtin_amdgcn_fence(__ATOMIC_ACQUIRE, "wavefront");
    }
    WAITL0();
    BAR();
    STAMP(14);
    bf16x8 a[8];
#pragma unroll
    for (int i = 0; i < 8; ++i)
      a[i] = __builtin_bit_cast(bf16x8, __builtin_amdgcn_raw_buffer_load_b128(rws, WS_ATT + ((4 * rt + mt) * 16 + kh * 8) * 1024 + lane * 16 + (i & 3) * 1024, (i >> 2) * 4096, AUX_SC1));
    v2u xr[2];
#pragma unroll
    for (int n = 0; n < 2; ++n) xr[n] = *(const GAS v2u*)(p.x + (size_t)row * D_ + 64 * ct + (2 * kh + n) * 16 + 4 * g);
    if (has3 && w == 0) issue_gu(0, GU_A);
    if (has3) issue_gu(1, GU_B);
    SB();
    f32x4 acc[4] = {z4, z4, z4, z4};
#pragma unroll
    for (int i = 0; i < 8; ++i)
#pragma unroll
      for (int n = 0; n < 4; ++n) acc[n] = mfma32(wf[i][n], a[i], acc[n]);
    STAMP(15);
    LAS f32x4* red = (LAS f32x4*)(L + RED);
#pragma unroll
    for (int n = 0; n < 2; ++n) red[((mt * 2 + kh) * 2 + n) * 64 + lane] = kh ? acc[n] : acc[2 + n];
    WAITL0();
    BAR();
    float s1 = 0.f, s2 = 0.f;
#pragma unroll
    for (int n = 0; n < 2; ++n) {
      const f32x4 o = red[((mt * 2 + (kh ^ 1)) * 2 + n) * 64 + lane];
      const f32x4 y = (kh ? acc[2 + n] : acc[n]) + o;
      const unsigned y01 = pack2(y[0], y[1]), y23 = pack2(y[2], y[3]);
      hp[n][0] = pack2(bflo(xr[n][0]) + bflo(y01), bfhi(xr[n][0]) + bfhi(y01));
      hp[n][1] = pack2(bflo(xr[n][1]) + bflo(y23), bfhi(xr[n][1]) + bfhi(y23));
      s1 = dot2(hp[n][0], o2, s1); s1 = dot2(hp[n][1], o2, s1);
      s2 = dot2(hp[n][0], hp[n][0], s2); s2 = dot2(hp[n][1], hp[n][1], s2);
    }
    {
      const auto a0 = __builtin_amdgcn_permlane16_swap(hp[0][0], hp[1][0], false, false);
      const auto a1 = __builtin_amdgcn_permlane16_swap(hp[0][1], hp[1][1], false, false);
      const v4u o = {a0[0], a1[0], a0[1], a1[1]};
      const int chunk = ((g & 1) << 1) | (g >> 1);
      __builtin_amdgcn_raw_buffer_store_b128(o, rws, WS_HF + ((4 * rt + mt) * 16 + 2 * ct + kh) * 1024 + (chunk * 16 + r) * 16, 0, AUX_SC1);
      s1 = xsum_g(s1); s2 = xsum_g(s2);
      if (lane < 16) {
        v2u sv = {asu(s1), asu(s2)};
        __builtin_amdgcn_raw_buffer_store_b64(sv, rws, WS_ST + (((4 * rt + mt) * 16 + 2 * ct + kh) * 16 + r) * 8, 0, AUX_SC1);
      }
    }
    STAMP(16);
    WAITV(0);
    BAR();
    if (tid == 0) __hip_atomic_fetch_add(fl + (FL_C2 + rt * 128) / 4, 1u, RLX_AGENT);
    STAMP(17);
  }
  const __amdgpu_buffer_rsrc_t rwd = mkrs(p.wd, F_ * D_ * 2);
  const unsigned dl = wdma_lane(lane, 1024, 0);
  auto issue_wd = [&](int c) {
    const int i = w * 2, half = i >> 3, ksl = (i >> 2) & 1, pk = i & 3;
    const unsigned so = ((half * 28 + 2 * c + ksl) * 32 + 8 * pk) * 1024 + c8 * 128;
    dma16(rwd, wd_slot(c) + i * 1024, dl, so);
    dma16(rwd, wd_slot(c) + i * 1024 + 1024, dl ^ 64, so + 8192);
  };
  if (has3) {
    {
      f32x4 cs = z4;
      const unsigned wb = (wtr_lane(lane, w & 3) ^ ((w >> 2) << 6)) + (w >> 2) * 4096;
      bf16x8 t[16];
#pragma unroll
      for (int ks = 0; ks < 16; ++ks) t[ks] = tr_frag(L + ks * 8192 + wb);
      SB();
#pragma unroll
      for (int ks = 0; ks < 16; ++ks) cs = mfma32(t[ks], ones, cs);
      if (r == 0) *(LAS f32x4*)(L + P3_CS + (w * 16 + 4 * g) * 4) = cs;
    }
    if (w != 0) issue_wd(0);
    STAMP(18);
    if (w == 0) {
      for (unsigned spins = 0;;) {
        const bool ok = lane >= 4 || __hip_atomic_load(fl + (FL_C2 + (4 * pn + (lane & 3)) * 128) / 4, RLX_AGENT) >= 8u;
        if (__all(ok)) break;
        if (poll_fail(spins, fl, lane)) break;
      }
      __builtin_amdgcn_fence(__ATOMIC_ACQUIRE, "wavefront");
    }
    WAITL0();
    BAR();
    STAMP(19);
    const int mt0 = 16 * pn + 2 * w, tk = lane & 31, hf = lane >> 5;
    bf16x8 af[32];
    v2u sp[8];
    const unsigned hsrc = WS_HF + (mt0 + (tk >> 4)) * 16384 + (hf * 16 + (tk & 15)) * 16;
    auto ldh = [&](int s) { af[s] = __builtin_bit_cast(bf16x8, __builtin_amdgcn_raw_buffer_load_b128(rws, hsrc + (s & 7) * 512, (s >> 3) * 4096, AUX_SC1)); };
    const unsigned ssrc = WS_ST + (((mt0 + (tk >> 4)) * 16 + 8 * hf) * 16 + (tk & 15)) * 8;
#pragma unroll
    for (int s = 0; s < 8; ++s) ldh(s);
    if (w == 0) issue_wd(0);
    f32x16 acc[4];
#pragma unroll
    for (int n = 0; n < 4; ++n)
#pragma unroll
      for (int i = 0; i < 16; ++i) acc[n][i] = 0.f;
    unsigned wl[4], wlb[4];
    {
      const int q = (lane & 15) >> 2, pp = lane & 3, bb = (lane >> 4) & 1;
#pragma unroll
      for (int n = 0; n < 4; ++n) {
        const int pos = (2 * n + bb) ^ (((q >> 1) << 1) | (hf << 2) | 0) ^ ((pp >> 1) << 2);
        wl[n] = (pp >> 1) * 4096 + hf * 1024 + q * 128 + pos * 16 + (pp & 1) * 8;
        wlb[n] = wl[n] + GU_B;
        asm volatile("" : "+v"(wl[n]), "+v"(wlb[n]));
      }
    }
    auto rd = [&](unsigned base, int off) { return __builtin_amdgcn_ds_read_tr16_b64_v4i16((LAS bf16x4*)((LAS unsigned char*)(size_t)base + off)); };
    bf16x4 wh[4][2];
    auto rdM = [&](int M, int h) {
      const int n = M < 64 ? (M & 3) : (M - 64) >> 4, s = M < 64 ? (M >> 2) : ((M - 64) & 15);
      wh[M & 3][h] = rd(M < 64 ? wl[n] : wlb[n], (s >> 1) * 8192 + (s & 1) * 2048 + h * 512);
    };
    auto mmM = [&](int M) {
      const int n = M < 64 ? (M & 3) : (M - 64) >> 4, s = M < 64 ? (M >> 2) : 16 + ((M - 64) & 15);
      acc[n] = __builtin_amdgcn_mfma_f32_32x32x16_bf16(__builtin_shufflevector(wh[M & 3][0], wh[M & 3][1], 0, 1, 2, 3, 4, 5, 6, 7), af[s], acc[n], 0, 0, 0);
    };
#pragma unroll
    for (int M = 0; M < 3; ++M) { rdM(M, 0); rdM(M, 1); }
    SB();
    static_for<0, 64>([&](auto MM) {
      constexpr int M = decltype(MM)::value;
      mmM(M);
      rdM(M + 3, 0); rdM(M + 3, 1);
      if ((M & 1) == 0 && 8 + (M >> 1) < 32) ldh(8 + (M >> 1));
      if (M >= 48 && (M & 1) == 0) sp[(M - 48) >> 1] = __builtin_amdgcn_raw_buffer_load_b64(rws, ssrc + (((M - 48) >> 1) & 7) * 128, 0, AUX_SC1);
      SB();
    });
    STAMP(20);
    WAITL0();
    BAR();
#pragma unroll
    for (int c = 1; c < 5; ++c) issue_wd(c);
    STAMP(21);
    float nmean, c1, c3, irs2;
    {
      float s1 = 0.f, s2 = 0.f;
#pragma unroll
      for (int i = 0; i < 8; ++i) { s1 += asf(sp[i][0]); s2 += asf(sp[i][1]); }
      float a, b;
      swap32(s1, a, b); s1 = a + b;
      swap32(s2, a, b); s2 = a + b;
      const float mn = s1 * (1.f / D_);
      irs2 = fmaxf(s2 * (1.f / D_) - mn * mn, 0.f) + LN_EPS;
      nmean = -mn;
      const float rs = __builtin_amdgcn_rsqf(irs2);
      c1 = -2.302208198f * rs; c3 = -0.1029432397f * rs * rs * rs;
    }
    SB();
    const unsigned actoff = WS_ACT + ((mt0 + (tk >> 4)) * 56 + 2 * fs) * 1024 + (hf * 16 + (tk & 15)) * 16;
    f32x4 cv[4];
    float eG[8], eT[8], eZ[8];
    unsigned ob[4];
    auto epi_step = [&](int n, int k) {
      const int st = k >> 3, v = k & 7, gi = (v >> 2) * 8 + (v & 3), ui = gi + 4;
      if (k == 0) {
        cv[0] = *(LAS f32x4*)(L + P3_CS + (n * 16 + 4 * hf) * 4); cv[1] = *(LAS f32x4*)(L + P3_CS + ((4 + n) * 16 + 4 * hf) * 4);
        cv[2] = *(LAS f32x4*)(L + P3_CS + (n * 16 + 8 + 4 * hf) * 4); cv[3] = *(LAS f32x4*)(L + P3_CS + ((4 + n) * 16 + 8 + 4 * hf) * 4);
      }
      else if (st == 1) eG[v] = fmaf(nmean, cv[(v >> 2) * 2][v & 3], acc[n][gi]);
      else if (st == 2) eT[v] = eG[v] * eG[v];
      else if (st == 3) eT[v] = fmaf(eT[v], c3, c1);
      else if (st == 4) eT[v] = eT[v] * eG[v];
      else if (st == 5) eT[v] = __builtin_amdgcn_exp2f(eT[v]);
      else if (st == 6) eZ[v] = fmaf(nmean, cv[(v >> 2) * 2 + 1][v & 3], acc[n][ui]);
      else if (st == 7) eT[v] = fmaf(eT[v], irs2, irs2);
      else if (st == 8) eZ[v] = eZ[v] * eG[v];
      else if (st == 9) eT[v] = __builtin_amdgcn_rcpf(eT[v]);
      else if (st == 10) eZ[v] = eZ[v] * eT[v];
      else if (k >= 88 && k < 92) ob[k - 88] = pack2(eZ[2 * (k - 88)], eZ[2 * (k - 88) + 1]);
      else if (k == 92) {
        const auto a0 = __builtin_amdgcn_permlane32_swap(ob[0], ob[2], false, false);
        const auto a1 = __builtin_amdgcn_permlane32_swap(ob[1], ob[3], false, false);
        const v4u ov = {a0[0], a1[0], a0[1], a1[1]};
        __builtin_amdgcn_raw_buffer_store_b128(ov, rws, actoff + (n >> 1) * 1024 + (n & 1) * 512, 0, AUX_SC1);
      }
    };
    static_for<64, 128>([&](auto MM) {
      constexpr int M = decltype(MM)::value;
      constexpr int n = (M - 64) >> 4, sl = (M - 64) & 15;
      mmM(M);
      if (M + 3 < 128) { rdM(M + 3, 0); rdM(M + 3, 1); }
      if (n > 0) {
        if (sl == 0) epi_step(n - 1, 0);
        else if (sl <= 14) {
#pragma unroll
          for (int j = 0; j < 6; ++j) epi_step(n - 1, 8 + 6 * (sl - 1) + j);
        } else epi_step(n - 1, 92);
      }
      SB();
    });
    WAITL0();
    BAR();
#pragma unroll
    for (int c = 5; c < 9; ++c) issue_wd(c);
    STAMP(22);
    epi_step(3, 0);
    static_for<8, 93>([&](auto KK) { epi_step(3, decltype(KK)::value); });
    STAMP(29);
    WAITV(0);
    BAR();
    if (tid == 0) __hip_atomic_fetch_add(fl + (FL_C3 + pn * 128) / 4, 1u, RLX_AGENT);
    STAMP(30);
  } else {
#pragma unroll
    for (int c = 0; c < 9; ++c) issue_wd(c);
    WAITV(0);
    BAR();
  }
  {
    const int mt = w & 3, kh = w >> 2, ct = c8;
    unsigned wl[4], wh[4], wx[4];
#pragma unroll
    for (int n = 0; n < 4; ++n) {
      wl[n] = wtr_lane(lane, n) + kh * 8192; wh[n] = wl[n] + 65536; wx[n] = wl[n] + 131072;
      asm volatile("" : "+v"(wl[n]), "+v"(wh[n]), "+v"(wx[n]));
    }
    auto wdfrag = [&](int c, int sx, int n) {
      const int sl = wd_slot(c);
      return sl >= 131072 ? tr_frag((LAS unsigned char*)(size_t)wx[n] + sx * 4096)
           : sl >= 65536 ? tr_frag((LAS unsigned char*)(size_t)wh[n] + (sl - 65536) + sx * 4096) : tr_frag((LAS unsigned char*)(size_t)wl[n] + sl + sx * 4096);
    };
    bf16x8 wr[8][4];
#pragma unroll
    for (int i = 0; i < 8; ++i)
#pragma unroll
      for (int n = 0; n < 4; ++n) wr[i][n] = wdfrag(i >> 1, i & 1, n);
    WAITL0();
    BAR();
    if (w != 0) { issue_wd(9); issue_wd(10); issue_wd(11); issue_wd(12); }
    if (w == 0) {
      for (unsigned spins = 0;;) {
        const bool ok = __hip_atomic_load(fl + (FL_C3 + bx * 128) / 4, RLX_AGENT) >= 28u;
        if (__all(ok)) break;
        if (poll_fail(spins, fl, lane)) break;
      }
      __builtin_amdgcn_fence(__ATOMIC_ACQUIRE, "wavefront");
      if (bi == 31 && lane == (bx & 1)) __hip_atomic_store(fl + (FL_E + bx * 256) / 4, tagv, RLX_AGENT);
    }
    BAR();
    STAMP(31);
    bf16x8 a[28];
    const unsigned asrc = WS_ACT + ((4 * rt + mt) * 56 + kh * 28) * 1024 + lane * 16;
    auto lda = [&](int i) { a[i] = __builtin_bit_cast(bf16x8, __builtin_amdgcn_raw_buffer_load_b128(rws, asrc + (i & 3) * 1024, (i >> 2) * 4096, AUX_SC1)); };
#pragma unroll
    for (int i = 0; i < 6; ++i) lda(i);
    if (w == 0) { issue_wd(9); issue_wd(10); issue_wd(11); issue_wd(12); }
    f32x4 acc[4] = {z4, z4, z4, z4};
    bf16x8 wf[2][4];
    SB();
#pragma unroll
    for (int i = 0; i < 28; ++i) {
      if (i == 10) { WAITL0(); BAR(); issue_wd(13); STAMP(32); }
      if (i == 25) { WAITV(0); WAITL0(); BAR(); }
      if (i + 6 < 28) lda(i + 6);
      if (i + 1 >= 8 && i + 1 < 28) {
        const int c = (i + 1) >> 1, sx = (i + 1) & 1;
#pragma unroll
        for (int n = 0; n < 4; ++n) wf[(i + 1) & 1][n] = wdfrag(c, sx, n);
      }
#pragma unroll
      for (int n = 0; n < 4; ++n) acc[n] = mfma32(i < 8 ? wr[i & 7][n] : wf[i & 1][n], a[i], acc[n]);
      if (i + 1 >= 8 && i + 1 < 28) {
#pragma unroll
        for (int n = 0; n < 4; ++n) { SG(M_MFMA, 1); SG(M_DSR, 2); }
      }
      SB();
    }
    WAITL0();
    BAR();
    STAMP(33);
    LAS f32x4* red = (LAS f32x4*)L;
#pragma unroll
    for (int n = 0; n < 2; ++n) red[((mt * 2 + kh) * 2 + n) * 64 + lane] = kh ? acc[n] : acc[2 + n];
    WAITL0();
    BAR();
    v2u op[2];
#pragma unroll
    for (int n = 0; n < 2; ++n) {
      const f32x4 o = red[((mt * 2 + (kh ^ 1)) * 2 + n) * 64 + lane];
      const f32x4 y = (kh ? acc[2 + n] : acc[n]) + o;
      const unsigned y01 = pack2(y[0], y[1]), y23 = pack2(y[2], y[3]);
      op[n][0] = pack2(bflo(hp[n][0]) + bflo(y01), bfhi(hp[n][0]) + bfhi(y01));
      op[n][1] = pack2(bflo(hp[n][1]) + bflo(y23), bfhi(hp[n][1]) + bfhi(y23));
    }
    const auto a0 = __builtin_amdgcn_permlane16_swap(op[0][0], op[1][0], false, false);
    const auto a1 = __builtin_amdgcn_permlane16_swap(op[0][1], op[1][1], false, false);
    const v4u o = {a0[0], a1[0], a0[1], a1[1]};
    const int chunk = ((g & 1) << 1) | (g >> 1);
    *(GAS v4u*)(p.out + (size_t)(64 * rt + 16 * mt + r) * D_ + 64 * ct + 32 * kh + chunk * 8) = o;
    STAMP(34);
  }
  STAMP_END();
}

#if defined(__HIP_DEVICE_COMPILE__)
#pragma clang attribute pop
#endif
extern "C" void kernel_launch(void* const* d_in, const int* in_sizes, int n_in, void* d_out, int out_size, void* d_ws, size_t ws_size, hipStream_t stream) {
  Params p;
  p.x = (const unsigned short*)d_in[0];
  p.wqkv = (const unsigned short*)d_in[1];
  p.wo = (const unsigned short*)d_in[2];
  p.wg = (const unsigned short*)d_in[4];
  p.wu = (const unsigned short*)d_in[5];
  p.wd = (const unsigned short*)d_in[6];
  p.out = (unsigned short*)d_out;
  p.ws = (unsigned char*)d_ws;
  constexpr int LDS = 160 * 1024;
  (void)hipFuncSetAttribute((const void*)k_fused, hipFuncAttributeMaxDynamicSharedMemorySize, LDS);
  k_fused<<<256, 512, LDS, stream>>>(p);
}
```

```cpp
#include <hip/hip_runtime.h>
#include <hip/hip_bf16.h>

#define GAS __attribute__((address_space(1)))
#define LAS __attribute__((address_space(3)))
typedef short bf16x8 __attribute__((ext_vector_type(8)));
typedef short bf16x4 __attribute__((ext_vector_type(4)));
typedef __bf16 bf2 __attribute__((ext_vector_type(2)));
typedef float f32x4 __attribute__((ext_vector_type(4)));
typedef float f32x2 __attribute__((ext_vector_type(2)));
typedef float f32x16 __attribute__((ext_vector_type(16)));
typedef unsigned v4u __attribute__((ext_vector_type(4)));
typedef unsigned v2u __attribute__((ext_vector_type(2)));
#define DI __device__ __forceinline__

constexpr int S_ = 2048, D_ = 512, F_ = 1792;
constexpr int AUX_SC1 = 16;
constexpr unsigned RSRC3 = 0x00020000u;
constexpr unsigned WS_ATT = 0u;
constexpr unsigned WS_HF = 2u << 20;
constexpr unsigned WS_ACT = 4u << 20;
constexpr unsigned WS_ST = 12u << 20;
constexpr unsigned WS_FL = 13u << 20;
constexpr unsigned FL_E = 0, FL_F1 = 4096, FL_C2 = 16384, FL_C3 = 24576, FL_TMO = 28672;
constexpr unsigned WS_BYTES = 14u << 20;
typedef GAS unsigned gu32;
#define RLX_AGENT __ATOMIC_RELAXED, __HIP_MEMORY_SCOPE_AGENT
constexpr unsigned SPIN_CAP = 1u << 18;

struct Params {
  const unsigned short* x;
  const unsigned short* wqkv;
  const unsigned short* wo;
  const unsigned short* wg;
  const unsigned short* wu;
  const unsigned short* wd;
  unsigned short* out;
  unsigned char* ws;
};

#if defined(__HIP_DEVICE_COMPILE__)
#pragma clang attribute push (__attribute__((target("no-packed-fp32-ops"))), apply_to = function)
#endif
template <int V> struct IC { static constexpr int value = V; };
template <int A, int B, class F> __device__ __forceinline__ void static_for(F&& f) { if constexpr (A < B) { f(IC<A>{}); static_for<A + 1, B>(f); } }
DI float asf(unsigned u) { return __builtin_bit_cast(float, u); }
DI unsigned asu(float f) { return __builtin_bit_cast(unsigned, f); }
DI f32x4 mfma32(bf16x8 a, bf16x8 b, f32x4 c) { return __builtin_amdgcn_mfma_f32_16x16x32_bf16(a, b, c, 0, 0, 0); }
DI f32x4 mfma16(bf16x4 a, bf16x4 b, f32x4 c) { return __builtin_amdgcn_mfma_f32_16x16x16bf16_1k(a, b, c, 0, 0, 0); }
DI unsigned pack2(float lo, float hi) { bf2 p; p[0] = (__bf16)lo; p[1] = (__bf16)hi; return __builtin_bit_cast(unsigned, p); }
DI float bflo(unsigned u) { return __builtin_bit_cast(float, u << 16); }
DI float bfhi(unsigned u) { return __builtin_bit_cast(float, u & 0xffff0000u); }
DI unsigned ones2() { unsigned o = 0x3F803F80u; asm volatile("" : "+v"(o)); return o; }
DI float dot2(unsigned a, unsigned b, float c) { return __builtin_amdgcn_fdot2_f32_bf16(__builtin_bit_cast(bf2, a), __builtin_bit_cast(bf2, b), c, false); }
DI __amdgpu_buffer_rsrc_t mkrs(const void* p, unsigned bytes) { return __builtin_amdgcn_make_buffer_rsrc((void*)p, 0, bytes, RSRC3); }
DI void dma16(__amdgpu_buffer_rsrc_t rs, unsigned dst, unsigned voff, unsigned soff) {
  asm volatile("s_mov_b32 m0, %0\n\ts_nop 0\n\tbuffer_load_dwordx4 %1, %2, %3 offen lds"
               :: "s"(dst), "v"(voff), "s"(rs), "s"(soff) : "m0", "memory");
}
template <int SS> DI void dma16x4(__amdgpu_buffer_rsrc_t rs, unsigned dst, unsigned v0, unsigned v1, unsigned soff) {
  unsigned t;
  asm volatile("s_mov_b32 m0, %1\n\ts_add_u32 %0, %5, %6\n\t"
               "buffer_load_dwordx4 %2, %4, %5 offen lds\n\t"
               "buffer_load_dwordx4 %3, %4, %0 offen offset:1024 lds\n\ts_add_u32 %0, %0, %6\n\t"
               "buffer_load_dwordx4 %2, %4, %0 offen offset:2048 lds\n\ts_add_u32 %0, %0, %6\n\t"
               "buffer_load_dwordx4 %3, %4, %0 offen offset:3072 lds"
               : "=&s"(t) : "s"(dst), "v"(v0), "v"(v1), "s"(rs), "s"(soff), "n"(SS - 1024) : "m0", "scc", "memory");
}
template <int LS> DI void dma16x3(__amdgpu_buffer_rsrc_t rs, unsigned dst, unsigned v, unsigned soff) {
  asm volatile("s_mov_b32 m0, %0\n\ts_nop 0\n\t"
               "buffer_load_dwordx4 %1, %2, %3 offen lds\n\ts_add_u32 m0, m0, %4\n\ts_nop 0\n\t"
               "buffer_load_dwordx4 %1, %2, %3 offen offset:1024 lds\n\ts_add_u32 m0, m0, %4\n\ts_nop 0\n\t"
               "buffer_load_dwordx4 %1, %2, %3 offen offset:2048 lds"
               :: "s"(dst), "v"(v), "s"(rs), "s"(soff), "n"(LS - 1024) : "m0", "scc", "memory");
}
DI bf16x8 lds_frag(LAS unsigned char* p) { return *(LAS bf16x8*)p; }
DI bf16x8 tr_frag(LAS unsigned char* p) {
  bf16x4 lo = __builtin_amdgcn_ds_read_tr16_b64_v4i16((LAS bf16x4*)p);
  bf16x4 hi = __builtin_amdgcn_ds_read_tr16_b64_v4i16((LAS bf16x4*)(p + 512));
  return __builtin_shufflevector(lo, hi, 0, 1, 2, 3, 4, 5, 6, 7);
}
DI void swap16(float v, float& a, float& b) {
  const unsigned u = asu(v);
  auto r = __builtin_amdgcn_permlane16_swap(u, u, false, false);
  a = asf(r[0]); b = asf(r[1]);
}
DI void swap32(float v, float& a, float& b) {
  const unsigned u = asu(v);
  auto r = __builtin_amdgcn_permlane32_swap(u, u, false, false);
  a = asf(r[0]); b = asf(r[1]);
}
DI float xsum_g(float v) {
  const f32x4 z = {0.f, 0.f, 0.f, 0.f};
  asm volatile("s_nop 3" : "+v"(v));
  return __builtin_amdgcn_mfma_f32_16x16x4f32(1.0f, v, z, 0, 0, 0)[0];
}
DI float xmax_g(float v) {
  float a, b;
  swap16(v, a, b); v = fmaxf(a, b);
  swap32(v, a, b); return fmaxf(a, b);
}
#define WAITV(n) asm volatile("s_waitcnt vmcnt(" #n ")" ::: "memory")
#define WAITL0() asm volatile("s_waitcnt lgkmcnt(0)" ::: "memory")
#define BAR() asm volatile("s_barrier" ::: "memory")
#define SB() __builtin_amdgcn_sched_barrier(0)
#define SG(mask, n) __builtin_amdgcn_sched_group_barrier(mask, n, 0)
#define M_MFMA 0x8
#define M_DSR 0x100
#define M_DSW 0x200
#define M_VALU 0x2
#define M_TRANS 0x400
#define M_VMEMR 0x20
constexpr float LN_EPS = 1e-5f;
#define STAMP_DECL do {} while (0)
#define STAMP(i) do {} while (0)
#define STAMP_END() do {} while (0)

DI unsigned wtr_lane(int lane, int nt) {
  const int g = lane >> 4, q = (lane & 15) >> 2, p = lane & 3;
  const int sg = (q >> 1) | ((g & 1) << 1);
  return g * 1024 + q * 128 + ((nt ^ sg) << 5) + p * 8;
}
DI unsigned wdma32_lane(int lane, unsigned pitch) {
  const int r8 = lane >> 3, pos = lane & 7;
  return r8 * pitch + ((pos ^ (((r8 >> 1) & 1) << 2)) << 4);
}
DI unsigned wdma_lane(int lane, unsigned pitch, int odd) {
  const int r8 = lane >> 3, pos = lane & 7;
  return r8 * pitch + ((pos ^ (((r8 >> 1) & 1) << 1) ^ (odd << 2)) << 4);
}

constexpr int P1_X = 0;
constexpr int P1_W = 81920;
constexpr int SM = 155648;
constexpr int P1_PART = SM;
constexpr int P1_CS = SM + 2560;
constexpr int P1_FIN = SM + 4096;
constexpr int LC = SM + 7168;
constexpr int P3_CS = SM;
constexpr int WO_IMG = 65536;
constexpr int RED = 131072;
constexpr int GU_A = 0;
constexpr int GU_B = 65536;
DI constexpr int wd_slot(int c) { return (c % 9) == 0 ? 131072 : ((c % 9) - 1) * 16384; }

DI bool poll_fail(unsigned& spins, gu32* fl, int lane) {
  if (++spins > SPIN_CAP) { if (lane == 0) __hip_atomic_store(fl + FL_TMO / 4, 1u, RLX_AGENT); return true; }
  return false;
}

__global__ __launch_bounds__(512, 2) void k_fused(Params p) {
  extern __shared__ __attribute__((aligned(16))) unsigned char smem[];
  LAS unsigned char* L = (LAS unsigned char*)smem;
  const int tid = threadIdx.x, lane = tid & 63, w = __builtin_amdgcn_readfirstlane(tid >> 6);
  const int bx = blockIdx.x & 7, bi = blockIdx.x >> 3, rt = 4 * bx + (bi >> 3), c8 = bi & 7;
  const int pr = bx >> 1, pc = bx & 1;
  const int r = lane & 15, g = lane >> 4;
  const __amdgpu_buffer_rsrc_t rws = mkrs(p.ws, WS_BYTES);
  gu32* fl = (gu32*)(p.ws + WS_FL);
  const f32x4 z4 = {0.f, 0.f, 0.f, 0.f};
  const bf16x8 ones = {0x3F80, 0x3F80, 0x3F80, 0x3F80, 0x3F80, 0x3F80, 0x3F80, 0x3F80};
  const unsigned o2 = ones2();
  const int rt1 = 8 * pr + (bi >> 2), hg = 4 * pc + (bi & 3);
  const bool has3 = bi < 28;
  const int pn = 2 * pr + (bi >= 14 ? 1 : 0), fs = 14 * pc + (bi >= 14 ? bi - 14 : bi);
  const int r2 = (c8 + 2 * (rt & 3)) & 7, r3 = fs & 7, fl3 = bi >= 14 ? 8 : 0, r4 = (2 * c8 + 3 * (rt & 3)) % 14;
  if (tid < 16) ((LAS unsigned*)(L + LC))[tid] = 0u;
  STAMP_DECL;
  STAMP(0);
  const unsigned tagv = __hip_atomic_load(fl + (FL_E + (2 * pr + (lane & 1)) * 256) / 4, RLX_AGENT) + 1u;
  if (w == 0) {
    if (hg == 0 && lane == 0) {
      __hip_atomic_store(fl + (FL_C2 + rt1 * 128) / 4, 0u, RLX_AGENT);
      if ((rt1 & 3) == 0) __hip_atomic_store(fl + (FL_C3 + (rt1 >> 2) * 128) / 4, 0u, RLX_AGENT);
    }
  }
  {
    const int hh = w & 3, rh = w >> 2, rot = (bi + (bi >> 2) + 2 * pr) & 7;
    const __amdgpu_buffer_rsrc_t rx = mkrs(p.x, S_ * D_ * 2), rw = mkrs(p.wqkv, D_ * 3 * D_ * 2);
    const int r8 = lane >> 3, pos = lane & 7;
    const unsigned xv0 = r8 * 1024 + ((pos ^ (r8 >> 1)) << 4), xv1 = xv0 ^ 64;
    auto issue_x = [&](int half) {
      const int c = 4 * half + (w >> 1);
#pragma unroll
      for (int i = 0; i < 5; ++i) {
        const int P = 5 * (w & 1) + i;
        int row = 64 * rt1 - 16 + 8 * P;
        row = row < 0 ? 0 : row;
        dma16(rx, P1_X + (c * 10 + P) * 1024, (P & 1) ? xv1 : xv0, row * 1024 + ((c + rot) & 7) * 128);
      }
    };
    const unsigned wv = wdma_lane(lane, 3072, w & 1);
    auto issue_w = [&](int c) {
      dma16x3<8192>(rw, P1_W + (c % 3) * 24576 + w * 1024, wv, (((c + rot) & 7) * 64 + 8 * w) * 3072 + hg * 128);
    };
    issue_x(0); issue_w(0);
    v4u wu_[3];
    {
      const unsigned b = blockIdx.x;
      const GAS v4u* q0 = w < 7 ? (const GAS v4u*)p.wg + b * 448 + w * 64 + lane : (const GAS v4u*)p.wo + b * 128 + lane;
      const GAS v4u* q1 = w < 7 ? (const GAS v4u*)p.wu + b * 448 + w * 64 + lane : (const GAS v4u*)p.wo + b * 128 + 64 + lane;
      const GAS v4u* q2 = (const GAS v4u*)p.wd + b * 448 + (w < 7 ? w : 6) * 64 + lane;
      asm volatile("global_load_dwordx4 %0, %3, off\n\tglobal_load_dwordx4 %1, %4, off\n\tglobal_load_dwordx4 %2, %5, off"
                   : "=&v"(wu_[0]), "=&v"(wu_[1]), "=&v"(wu_[2]) : "v"(q0), "v"(q1), "v"(q2) : "memory");
    }
    issue_w(1); issue_w(2);
    STAMP(1);
    f32x4 aq[2] = {z4, z4}, ak[3] = {z4, z4, z4}, av[3] = {z4, z4, z4}, aqh = z4;
    float s1 = 0.f, s2 = 0.f;
    const unsigned xlane = (r >> 3) * 1024 + (r & 7) * 128 + ((g ^ ((r >> 1) & 7)) << 4);
    unsigned xl0 = xlane + 2 * rh * 2048, xl1 = xl0 ^ 64;
    const int stile = rh == 0 ? (hh < 3 ? hh : 0) : (hh < 2 ? 3 + hh : 3);
    unsigned xs0 = xlane + stile * 2048, xs1 = xs0 ^ 64;
    unsigned wlA = wtr_lane(lane, hh) + P1_W, wlB = wlA + 49152;
    unsigned xh0 = xl0 + 40960, xh1 = xl1 + 40960, xsh0 = xs0 + 40960, xsh1 = xs1 + 40960;
    asm volatile("" : "+v"(xl0), "+v"(xl1), "+v"(xs0), "+v"(xs1), "+v"(wlA), "+v"(wlB), "+v"(xh0), "+v"(xh1), "+v"(xsh0), "+v"(xsh1));
    bf16x8 fx[2][4];
    bf16x4 fw[2][3][2];
    auto rd1 = [&](int K, int j) {
      const int c = K >> 1, sx = K & 1, bf = K & 1;
      if (j < 6) {
        const int part = j >> 1, h = j & 1;
        const unsigned base = (c % 3) == 2 ? wlB : wlA;
        const int off = ((c % 3) == 2 ? 0 : (c % 3) * 24576) + part * 8192 + sx * 4096 + h * 512;
        fw[bf][part][h] = __builtin_amdgcn_ds_read_tr16_b64_v4i16((LAS bf16x4*)((LAS unsigned char*)(size_t)base + off));
      } else if (j < 9) {
        const int t = j == 6 ? 1 : j == 7 ? 2 : 0;
        const unsigned base = c < 4 ? (sx ? xl1 : xl0) : (sx ? xh1 : xh0);
        fx[bf][t] = lds_frag((LAS unsigned char*)(size_t)base + (c & 3) * 10240 + t * 2048);
      } else {
        const unsigned base = c < 4 ? (sx ? xs1 : xs0) : (sx ? xsh1 : xsh0);
        fx[bf][3] = lds_frag((LAS unsigned char*)(size_t)base + (c & 3) * 10240);
      }
    };
    constexpr int RORD[10] = {0, 1, 6, 7, 2, 3, 8, 4, 5, 9};
    auto wfrag = [&](int bf, int part) { return __builtin_shufflevector(fw[bf][part][0], fw[bf][part][1], 0, 1, 2, 3, 4, 5, 6, 7); };
    auto mm1 = [&](int K, int j) {
      const int bf = K & 1;
      if (j == 0) aq[0] = mfma32(wfrag(bf, 0), fx[bf][1], aq[0]);
      else if (j == 1) aq[1] = mfma32(wfrag(bf, 0), fx[bf][2], aq[1]);
      else if (j == 2) ak[1] = mfma32(wfrag(bf, 1), fx[bf][1], ak[1]);
      else if (j == 3) ak[2] = mfma32(wfrag(bf, 1), fx[bf][2], ak[2]);
      else if (j == 4) ak[0] = mfma32(wfrag(bf, 1), fx[bf][0], ak[0]);
      else if (j == 5) av[0] = mfma32(fx[bf][0], wfrag(bf, 2), av[0]);
      else if (j == 6) av[1] = mfma32(fx[bf][1], wfrag(bf, 2), av[1]);
      else av[2] = mfma32(fx[bf][2], wfrag(bf, 2), av[2]);
    };
    auto ones_row = [&](int half) {
      if ((w & 1) == 0 && lane < 8) *(LAS v4u*)(L + P1_X + (4 * half + (w >> 1)) * 10240 + lane * 16) = v4u{0x3F803F80u, 0x3F803F80u, 0x3F803F80u, 0x3F803F80u};
    };
    WAITV(9);
    ones_row(0);
    WAITL0();
    BAR();
    STAMP(2);
#pragma unroll
    for (int j = 0; j < 10; ++j) rd1(0, RORD[j]);
    SB();
    static_for<0, 16>([&](auto KK) {
      constexpr int K = decltype(KK)::value, c = K >> 1;
      if constexpr ((K & 1) == 1 && c < 7) {
        if (c == 1 || c == 2) WAITV(8);
        else if (c == 6) WAITV(0);
        else WAITV(3);
        if (c == 0) asm volatile("" :: "v"(wu_[0]), "v"(wu_[1]), "v"(wu_[2]));
        if (c == 3) ones_row(1);
        WAITL0();
        BAR();
        STAMP(3 + c);
        if (c + 3 < 8) issue_w(c + 3);
        if (c == 0) issue_x(1);
        SB();
      }
      static_for<0, 8>([&](auto JJ) {
        constexpr int j = decltype(JJ)::value;
        mm1(K, j);
        if (K + 1 < 16) { rd1(K + 1, RORD[j]); if (j >= 6) rd1(K + 1, RORD[j + 2]); }
        { const v4u u = __builtin_bit_cast(v4u, fx[K & 1][3]);
          if (j & 1) s2 = dot2(u[j >> 1], u[j >> 1], s2); else s1 = dot2(u[j >> 1], o2, s1); }
        SB();
      });
      if (rh == 0) aqh = mfma32(wfrag(K & 1, 0), fx[K & 1][0], aqh);
      SB();
    });
    STAMP(10);
    LAS f32x2* fin = (LAS f32x2*)(L + P1_FIN);
    LAS float* csx = (LAS float*)(L + P1_CS);
    {
      const float a = xsum_g(s1), b = xsum_g(s2);
      const float mean = a * (1.f / D_);
      const float rstd = __builtin_amdgcn_rsqf(fmaxf(b * (1.f / D_) - mean * mean, 0.f) + LN_EPS);
      if (lane < 16 && (rh == 0 ? hh < 3 : hh < 2)) fin[stile * 16 + lane] = f32x2{mean, rstd};
      if (rh == 0) {
        if (r == 0) { *(LAS f32x4*)&csx[hh * 48 + 4 * g] = aqh; *(LAS f32x4*)&csx[hh * 48 + 16 + 4 * g] = ak[0]; }
        if (lane < 16) csx[hh * 48 + 32 + lane] = av[0][0];
      }
    }
    WAITL0();
    BAR();
    {
      const __amdgpu_buffer_rsrc_t rwo = mkrs(p.wo, D_ * D_ * 2);
      const unsigned a0 = wdma32_lane(lane, 1024), a1 = a0;
      dma16x4<8192>(rwo, WO_IMG + w * 8192, a0, a1, (((2 * w) & 8) | ((2 * w + r2) & 7)) * 32768 + c8 * 128);
      dma16x4<8192>(rwo, WO_IMG + w * 8192 + 4096, a0, a1, (((2 * w) & 8) | ((2 * w + 1 + r2) & 7)) * 32768 + c8 * 128);
    }
    const f32x4 cq = *(LAS f32x4*)&csx[hh * 48 + 4 * g], ck = *(LAS f32x4*)&csx[hh * 48 + 16 + 4 * g];
    const float cv0 = csx[hh * 48 + 32 + r];
    STAMP(11);
    bf16x4 Qp[2], Kp[3], Vp[3];
#pragma unroll
    for (int t = 0; t < 3; ++t) {
      const f32x2 st = fin[(2 * rh + t) * 16 + r];
      const f32x4 s01 = *(LAS f32x4*)&fin[(2 * rh + t) * 16 + 4 * g], s23 = *(LAS f32x4*)&fin[(2 * rh + t) * 16 + 4 * g + 2];
      const float nm = -st[0], rs = st[1];
      v2u u;
      u[0] = pack2(fmaf(nm, ck[0], ak[t][0]) * rs, fmaf(nm, ck[1], ak[t][1]) * rs);
      u[1] = pack2(fmaf(nm, ck[2], ak[t][2]) * rs, fmaf(nm, ck[3], ak[t][3]) * rs);
      Kp[t] = __builtin_bit_cast(bf16x4, u);
      if (t >= 1) {
        u[0] = pack2(fmaf(nm, cq[0], aq[t - 1][0]) * rs, fmaf(nm, cq[1], aq[t - 1][1]) * rs);
        u[1] = pack2(fmaf(nm, cq[2], aq[t - 1][2]) * rs, fmaf(nm, cq[3], aq[t - 1][3]) * rs);
        Qp[t - 1] = __builtin_bit_cast(bf16x4, u);
      }
      u[0] = pack2(fmaf(-s01[0], cv0, av[t][0]) * s01[1], fmaf(-s01[2], cv0, av[t][1]) * s01[3]);
      u[1] = pack2(fmaf(-s23[0], cv0, av[t][2]) * s23[1], fmaf(-s23[2], cv0, av[t][3]) * s23[3]);
      Vp[t] = __builtin_bit_cast(bf16x4, u);
    }
    f32x4 b0, b1;
#pragma unroll
    for (int i = 0; i < 4; ++i) {
      const int d0 = 16 + r - (4 * g + i), d1 = r - (4 * g + i);
      b0[i] = (d0 <= 14 && !(d0 & 1)) ? 0.f : -1.0e30f;
      b1[i] = (d1 >= 0 && d1 <= 14 && !(d1 & 1)) ? 0.f : -1.0e30f;
    }
    v2u O[2];
#pragma unroll
    for (int qt = 0; qt < 2; ++qt) {
      f32x4 bb = b0;
      if (qt == 0 && rt1 == 0 && rh == 0) bb = f32x4{-1.0e30f, -1.0e30f, -1.0e30f, -1.0e30f};
      const f32x4 S0 = mfma16(Kp[qt], Qp[qt], bb), S1 = mfma16(Kp[qt + 1], Qp[qt], b1);
      float m = fmaxf(fmaxf(fmaxf(S0[0], S0[1]), fmaxf(S0[2], S0[3])), fmaxf(fmaxf(S1[0], S1[1]), fmaxf(S1[2], S1[3])));
      m = xmax_g(m);
      constexpr float SC = 0.25f * 1.4426950408889634f;
      const float ms = -m * SC;
      float v[8];
#pragma unroll
      for (int i = 0; i < 4; ++i) { v[i] = __builtin_amdgcn_exp2f(fmaf(S0[i], SC, ms)); v[4 + i] = __builtin_amdgcn_exp2f(fmaf(S1[i], SC, ms)); }
      float sum = ((v[0] + v[1]) + (v[2] + v[3])) + ((v[4] + v[5]) + (v[6] + v[7]));
      sum = xsum_g(sum);
      const float inv = __builtin_amdgcn_rcpf(sum);
      v2u p0, p1;
      p0[0] = pack2(v[0] * inv, v[1] * inv); p0[1] = pack2(v[2] * inv, v[3] * inv);
      p1[0] = pack2(v[4] * inv, v[5] * inv); p1[1] = pack2(v[6] * inv, v[7] * inv);
      f32x4 o = mfma16(Vp[qt], __builtin_bit_cast(bf16x4, p0), z4);
      o = mfma16(Vp[qt + 1], __builtin_bit_cast(bf16x4, p1), o);
      O[qt][0] = pack2(o[0], o[1]); O[qt][1] = pack2(o[2], o[3]);
    }
    {
      const auto a = __builtin_amdgcn_permlane16_swap(O[0][0], O[1][0], false, false);
      const auto b = __builtin_amdgcn_permlane16_swap(O[0][1], O[1][1], false, false);
      const v4u o = {a[0], b[0], a[1], b[1]};
      const int head = 4 * hg + hh;
      const unsigned off = WS_ATT + ((4 * rt1 + 2 * rh + (g & 1)) * 16 + (head >> 1)) * 1024 + ((2 * (head & 1) + (g >> 1)) * 16 + r) * 16;
      __builtin_amdgcn_raw_buffer_store_b128(o, rws, off, 0, AUX_SC1);
    }
    STAMP(12);
    WAITV(0);
    BAR();
    if (tid == ((rt1 >> 2) & 1)) __hip_atomic_store(fl + (FL_F1 + rt1 * 128) / 4 + hg, tagv, RLX_AGENT);
    STAMP(13);
  }
  const __amdgpu_buffer_rsrc_t rg = mkrs(p.wg, D_ * F_ * 2), ru = mkrs(p.wu, D_ * F_ * 2);
  const unsigned gl0 = wdma_lane(lane, F_ * 2, 0), gl1 = gl0 ^ 64;
#define HD 63
#define THR() asm volatile("s_waitcnt vmcnt(%0)" :: "n"(HD) : "memory")
  auto issue_gu2 = [&](int half, int ks, int dst) {
    const unsigned so = ((((8 * half) ^ fl3) | ((ks + r3) & 7)) * 32) * (F_ * 2) + fs * 128;
    dma16x4<8 * F_ * 2>(rg, dst + ks * 8192, gl0, gl1, so);
    THR();
    dma16x4<8 * F_ * 2>(ru, dst + ks * 8192 + 4096, gl1, gl0, so);
    THR();
  };
  auto issue_gu = [&](int half, int dst) {
    const unsigned so = ((((8 * half) ^ fl3) | ((w + r3) & 7)) * 32) * (F_ * 2) + fs * 128;
    dma16x4<8 * F_ * 2>(rg, dst + w * 8192, gl0, gl1, so);
    dma16x4<8 * F_ * 2>(ru, dst + w * 8192 + 4096, gl1, gl0, so);
  };
  const __amdgpu_buffer_rsrc_t rwd = mkrs(p.wd, F_ * D_ * 2);
  const unsigned dl = wdma32_lane(lane, 1024);
  auto issue_wd4 = [&](int c, int qd) {
    const int pc_ = c + r4 >= 14 ? c + r4 - 14 : c + r4;
    dma16x4<8192>(rwd, wd_slot(c) + qd * 4096, dl, dl, (((qd >> 1) * 28 + 2 * pc_ + (qd & 1)) * 32) * 1024 + c8 * 128);
    THR();
  };
  auto issue_wd = [&](int c) {
    const int i = w * 2, half = i >> 3, ksl = (i >> 2) & 1, pk = i & 3;
    const int pc_ = c + r4 >= 14 ? c + r4 - 14 : c + r4;
    const unsigned so = ((half * 28 + 2 * pc_ + ksl) * 32 + 8 * pk) * 1024 + c8 * 128;
    dma16(rwd, wd_slot(c) + i * 1024, dl, so);
    dma16(rwd, wd_slot(c) + i * 1024 + 1024, dl, so + 8192);
  };
  unsigned hp[8];
  const int tk = lane & 31, hf = lane >> 5, mh = w & 1, kh = (w >> 1) & 1;
  unsigned wt[2];
  {
    const int q = (lane & 15) >> 2, pp = lane & 3, bb = (lane >> 4) & 1;
#pragma unroll
    for (int T = 0; T < 2; ++T) wt[T] = hf * 1024 + q * 128 + (((4 * T + 2 * bb + (pp >> 1)) ^ ((q >> 1) << 2)) << 4) + (pp & 1) * 8;
  }
  LAS unsigned* lc = (LAS unsigned*)(L + LC);
  if (w >= 4) {
    if (has3) {
      issue_gu2(0, 2 * (w - 4), GU_A); issue_gu2(0, 2 * (w - 4) + 1, GU_A);
      while (__hip_atomic_load(lc, __ATOMIC_RELAXED, __HIP_MEMORY_SCOPE_WORKGROUP) < 4u) __builtin_amdgcn_s_sleep(1);
      issue_gu2(1, 2 * (w - 4), GU_B); issue_gu2(1, 2 * (w - 4) + 1, GU_B);
    } else {
      issue_wd4(1, w - 4); issue_wd4(2, w - 4); issue_wd4(3, w - 4); issue_wd4(4, w - 4);
    }
  } else {
    const int ct = c8;
    const int row = 64 * rt + 32 * mh + tk;
    v2u xr[4];
#pragma unroll
    for (int j = 0; j < 4; ++j) xr[j] = *(const GAS v2u*)(p.x + (size_t)row * D_ + 64 * ct + 32 * kh + 8 * j + 4 * hf);
    bf16x8 wf[16][2];
    {
      unsigned wb[2] = {wt[0] + WO_IMG + kh * 32768, wt[1] + WO_IMG + kh * 32768};
      asm volatile("" : "+v"(wb[0]), "+v"(wb[1]));
#pragma unroll
      for (int sx = 0; sx < 16; ++sx)
#pragma unroll
        for (int T = 0; T < 2; ++T) wf[sx][T] = tr_frag((LAS unsigned char*)(size_t)wb[T] + (sx >> 1) * 4096 + (sx & 1) * 2048);
    }
    WAITL0();
    if (lane == 0) __hip_atomic_fetch_add(lc, 1u, __ATOMIC_RELAXED, __HIP_MEMORY_SCOPE_WORKGROUP);
    {
      const int frt = lane < 8 ? rt : lane < 12 ? 4 * bx + (lane & 3) : 4 * pn + (lane & 3);
      const unsigned idx = (FL_F1 + frt * 128) / 4 + (lane < 8 ? lane : 0);
      const unsigned tg = __shfl(tagv, (frt >> 2) & 1);
      for (unsigned spins = 0;;) {
        const bool ok = lane >= 16 || __hip_atomic_load(fl + idx, RLX_AGENT) == tg;
        if (__all(ok)) break;
        if (poll_fail(spins, fl, lane)) break;
      }
      __builtin_amdgcn_fence(__ATOMIC_ACQUIRE, "wavefront");
    }
    STAMP(14);
    bf16x8 a[16];
    {
      const unsigned av_ = WS_ATT + ((4 * rt + 2 * mh + (tk >> 4)) * 16 + kh * 8) * 1024 + (hf * 16 + (tk & 15)) * 16;
#pragma unroll
      for (int sx = 0; sx < 16; ++sx)
        a[sx] = __builtin_bit_cast(bf16x8, __builtin_amdgcn_raw_buffer_load_b128(rws, av_ + (sx & 1) * 512, (((sx >> 1) + r2) & 7) * 1024, AUX_SC1));
    }
    SB();
    f32x16 acc[2];
#pragma unroll
    for (int i = 0; i < 16; ++i) { acc[0][i] = 0.f; acc[1][i] = 0.f; }
#pragma unroll
    for (int sx = 0; sx < 16; ++sx)
#pragma unroll
      for (int T = 0; T < 2; ++T) acc[T] = __builtin_amdgcn_mfma_f32_32x32x16_bf16(wf[sx][T], a[sx], acc[T], 0, 0, 0);
    STAMP(15);
    LAS f32x4* red = (LAS f32x4*)(L + RED);
#pragma unroll
    for (int j = 0; j < 4; ++j) {
      const f32x16& o = acc[kh ^ 1];
      red[(((mh * 2 + kh) * 4) + j) * 64 + lane] = f32x4{o[4 * j], o[4 * j + 1], o[4 * j + 2], o[4 * j + 3]};
    }
    WAITL0();
    if (lane == 0) __hip_atomic_fetch_add(lc + 2, 1u, __ATOMIC_RELAXED, __HIP_MEMORY_SCOPE_WORKGROUP);
    while (__hip_atomic_load(lc + 2, __ATOMIC_RELAXED, __HIP_MEMORY_SCOPE_WORKGROUP) < 4u) {}
    float s1 = 0.f, s2 = 0.f;
#pragma unroll
    for (int j = 0; j < 4; ++j) {
      const f32x4 o = red[(((mh * 2 + (kh ^ 1)) * 4) + j) * 64 + lane];
      const f32x16& m = acc[kh];
      const unsigned y01 = pack2(m[4 * j] + o[0], m[4 * j + 1] + o[1]), y23 = pack2(m[4 * j + 2] + o[2], m[4 * j + 3] + o[3]);
      hp[2 * j] = pack2(bflo(xr[j][0]) + bflo(y01), bfhi(xr[j][0]) + bfhi(y01));
      hp[2 * j + 1] = pack2(bflo(xr[j][1]) + bflo(y23), bfhi(xr[j][1]) + bfhi(y23));
      s1 = dot2(hp[2 * j], o2, s1); s1 = dot2(hp[2 * j + 1], o2, s1);
      s2 = dot2(hp[2 * j], hp[2 * j], s2); s2 = dot2(hp[2 * j + 1], hp[2 * j + 1], s2);
    }
    {
      const unsigned ho = WS_HF + ((4 * rt + 2 * mh + (tk >> 4)) * 16 + 2 * ct + kh) * 1024 + (hf * 16 + (tk & 15)) * 16;
#pragma unroll
      for (int jj = 0; jj < 2; ++jj) {
        const auto a0 = __builtin_amdgcn_permlane32_swap(hp[4 * jj], hp[4 * jj + 2], false, false);
        const auto a1 = __builtin_amdgcn_permlane32_swap(hp[4 * jj + 1], hp[4 * jj + 3], false, false);
        const v4u o = {a0[0], a1[0], a0[1], a1[1]};
        __builtin_amdgcn_raw_buffer_store_b128(o, rws, ho + jj * 512, 0, AUX_SC1);
      }
      asm volatile("s_nop 3" : "+v"(s1), "+v"(s2));
      float x0, x1;
      swap32(s1, x0, x1); s1 = x0 + x1;
      swap32(s2, x0, x1); s2 = x0 + x1;
      if (lane < 32) {
        v2u sv = {asu(s1), asu(s2)};
        __builtin_amdgcn_raw_buffer_store_b64(sv, rws, WS_ST + (((4 * rt + 2 * mh + (tk >> 4)) * 16 + 2 * ct + kh) * 16 + (tk & 15)) * 8, 0, AUX_SC1);
      }
    }
    STAMP(16);
    WAITV(0);
    if (lane == 0) {
      const unsigned old = __hip_atomic_fetch_add(lc + 1, 1u, __ATOMIC_RELAXED, __HIP_MEMORY_SCOPE_WORKGROUP);
      if (old == 3u) __hip_atomic_fetch_add(fl + (FL_C2 + rt * 128) / 4, 1u, RLX_AGENT);
    }
    STAMP(17);
  }
  WAITV(0);
  WAITL0();
  BAR();
  if (has3) {
    {
      f32x4 cs = z4;
      const unsigned wb = (wtr_lane(lane, w & 3) ^ ((w >> 2) << 6)) + (w >> 2) * 4096;
      bf16x8 t[16];
#pragma unroll
      for (int ks = 0; ks < 16; ++ks) t[ks] = tr_frag(L + ks * 8192 + wb);
      SB();
#pragma unroll
      for (int ks = 0; ks < 16; ++ks) cs = mfma32(t[ks], ones, cs);
      if (r == 0) *(LAS f32x4*)(L + P3_CS + (w * 16 + 4 * g) * 4) = cs;
    }
    if (w != 0) issue_wd(0);
    STAMP(18);
    if (w == 0) {
      for (unsigned spins = 0;;) {
        const bool ok = lane >= 4 || __hip_atomic_load(fl + (FL_C2 + (4 * pn + (lane & 3)) * 128) / 4, RLX_AGENT) >= 8u;
        if (__all(ok)) break;
        if (poll_fail(spins, fl, lane)) break;
      }
      __builtin_amdgcn_fence(__ATOMIC_ACQUIRE, "wavefront");
    }
    WAITL0();
    BAR();
    STAMP(19);
    const int mt0 = 16 * pn + 2 * w, tk = lane & 31, hf = lane >> 5;
    bf16x8 af[32];
    v2u sp[8];
    const unsigned hsrc = WS_HF + (mt0 + (tk >> 4)) * 16384 + (hf * 16 + (tk & 15)) * 16;
    auto ldh = [&](int s) { af[s] = __builtin_bit_cast(bf16x8, __builtin_amdgcn_raw_buffer_load_b128(rws, hsrc + (s & 1) * 512, ((((s >> 1) & 8) ^ fl3) | (((s >> 1) + r3) & 7)) * 1024, AUX_SC1)); };
    const unsigned ssrc = WS_ST + (((mt0 + (tk >> 4)) * 16 + 8 * hf) * 16 + (tk & 15)) * 8;
#pragma unroll
    for (int s = 0; s < 8; ++s) ldh(s);
    if (w == 0) issue_wd(0);
    f32x16 acc[4];
#pragma unroll
    for (int n = 0; n < 4; ++n)
#pragma unroll
      for (int i = 0; i < 16; ++i) acc[n][i] = 0.f;
    unsigned wl[4], wlb[4];
    {
      const int q = (lane & 15) >> 2, pp = lane & 3, bb = (lane >> 4) & 1;
#pragma unroll
      for (int n = 0; n < 4; ++n) {
        const int pos = (2 * n + bb) ^ (((q >> 1) << 1) | (hf << 2) | 0) ^ ((pp >> 1) << 2);
        wl[n] = (pp >> 1) * 4096 + hf * 1024 + q * 128 + pos * 16 + (pp & 1) * 8;
        wlb[n] = wl[n] + GU_B;
        asm volatile("" : "+v"(wl[n]), "+v"(wlb[n]));
      }
    }
    auto rd = [&](unsigned base, int off) { return __builtin_amdgcn_ds_read_tr16_b64_v4i16((LAS bf16x4*)((LAS unsigned char*)(size_t)base + off)); };
    bf16x4 wh[4][2];
    auto rdM = [&](int M, int h) {
      const int n = M < 64 ? (M & 3) : (M - 64) >> 4, s = M < 64 ? (M >> 2) : ((M - 64) & 15);
      wh[M & 3][h] = rd(M < 64 ? wl[n] : wlb[n], (s >> 1) * 8192 + (s & 1) * 2048 + h * 512);
    };
    auto mmM = [&](int M) {
      const int n = M < 64 ? (M & 3) : (M - 64) >> 4, s = M < 64 ? (M >> 2) : 16 + ((M - 64) & 15);
      acc[n] = __builtin_amdgcn_mfma_f32_32x32x16_bf16(__builtin_shufflevector(wh[M & 3][0], wh[M & 3][1], 0, 1, 2, 3, 4, 5, 6, 7), af[s], acc[n], 0, 0, 0);
    };
#pragma unroll
    for (int M = 0; M < 3; ++M) { rdM(M, 0); rdM(M, 1); }
    SB();
    static_for<0, 64>([&](auto MM) {
      constexpr int M = decltype(MM)::value;
      mmM(M);
      rdM(M + 3, 0); rdM(M + 3, 1);
      if ((M & 1) == 0 && 8 + (M >> 1) < 32) ldh(8 + (M >> 1));
      if (M >= 48 && (M & 1) == 0) sp[(M - 48) >> 1] = __builtin_amdgcn_raw_buffer_load_b64(rws, ssrc + (((M - 48) >> 1) & 7) * 128, 0, AUX_SC1);
      SB();
    });
    STAMP(20);
    float nmean, c1, c3, irs2;
    {
      float s1 = 0.f, s2 = 0.f;
#pragma unroll
      for (int i = 0; i < 8; ++i) { s1 += asf(sp[i][0]); s2 += asf(sp[i][1]); }
      float a, b;
      swap32(s1, a, b); s1 = a + b;
      swap32(s2, a, b); s2 = a + b;
      const float mn = s1 * (1.f / D_);
      irs2 = fmaxf(s2 * (1.f / D_) - mn * mn, 0.f) + LN_EPS;
      nmean = -mn;
      const float rs = __builtin_amdgcn_rsqf(irs2);
      c1 = -2.302208198f * rs; c3 = -0.1029432397f * rs * rs * rs;
      asm volatile("" : "+v"(nmean), "+v"(c1), "+v"(c3), "+v"(irs2));
    }
    WAITL0();
    BAR();
#pragma unroll
    for (int c = 1; c < 5; ++c) issue_wd(c);
    STAMP(21);
    SB();
    const unsigned actoff = WS_ACT + ((mt0 + (tk >> 4)) * 56 + 2 * fs) * 1024 + (hf * 16 + (tk & 15)) * 16;
    f32x4 cv[4];
    float eG[8], eT[8], eZ[8];
    unsigned ob[4];
    auto epi_step = [&](int n, int k) {
      const int st = k >> 3, v = k & 7, gi = (v >> 2) * 8 + (v & 3), ui = gi + 4;
      if (k == 0) {
        cv[0] = *(LAS f32x4*)(L + P3_CS + (n * 16 + 4 * hf) * 4); cv[1] = *(LAS f32x4*)(L + P3_CS + ((4 + n) * 16 + 4 * hf) * 4);
        cv[2] = *(LAS f32x4*)(L + P3_CS + (n * 16 + 8 + 4 * hf) * 4); cv[3] = *(LAS f32x4*)(L + P3_CS + ((4 + n) * 16 + 8 + 4 * hf) * 4);
      }
      else if (st == 1) eG[v] = fmaf(nmean, cv[(v >> 2) * 2][v & 3], acc[n][gi]);
      else if (st == 2) eT[v] = eG[v] * eG[v];
      else if (st == 3) eT[v] = fmaf(eT[v], c3, c1);
      else if (st == 4) eT[v] = eT[v] * eG[v];
      else if (st == 5) eT[v] = __builtin_amdgcn_exp2f(eT[v]);
      else if (st == 6) eZ[v] = fmaf(nmean, cv[(v >> 2) * 2 + 1][v & 3], acc[n][ui]);
      else if (st == 7) eT[v] = fmaf(eT[v], irs2, irs2);
      else if (st == 8) eZ[v] = eZ[v] * eG[v];
      else if (st == 9) eT[v] = __builtin_amdgcn_rcpf(eT[v]);
      else if (st == 10) eZ[v] = eZ[v] * eT[v];
      else if (k >= 88 && k < 92) ob[k - 88] = pack2(eZ[2 * (k - 88)], eZ[2 * (k - 88) + 1]);
      else if (k == 92) {
        const auto a0 = __builtin_amdgcn_permlane32_swap(ob[0], ob[2], false, false);
        const auto a1 = __builtin_amdgcn_permlane32_swap(ob[1], ob[3], false, false);
        const v4u ov = {a0[0], a1[0], a0[1], a1[1]};
        __builtin_amdgcn_raw_buffer_store_b128(ov, rws, actoff + (n >> 1) * 1024 + (n & 1) * 512, 0, AUX_SC1);
      }
    };
    static_for<64, 128>([&](auto MM) {
      constexpr int M = decltype(MM)::value;
      constexpr int n = (M - 64) >> 4, sl = (M - 64) & 15;
      mmM(M);
      if (M + 3 < 128) { rdM(M + 3, 0); rdM(M + 3, 1); }
      if (n > 0) {
        if (sl == 0) epi_step(n - 1, 0);
        else if (sl <= 14) {
#pragma unroll
          for (int j = 0; j < 6; ++j) epi_step(n - 1, 8 + 6 * (sl - 1) + j);
        } else epi_step(n - 1, 92);
      }
      SB();
    });
    STAMP(22);
    epi_step(3, 0);
    static_for<8, 93>([&](auto KK) { epi_step(3, decltype(KK)::value); });
    STAMP(29);
    WAITV(0);
    BAR();
    if (tid == 0) __hip_atomic_fetch_add(fl + (FL_C3 + pn * 128) / 4, 1u, RLX_AGENT);
    STAMP(30);
  } else {
    issue_wd(0);
    WAITV(0);
    BAR();
  }
#define LC_ADD(i) do { WAITL0(); if (lane == 0) __hip_atomic_fetch_add(lc + (i), 1u, __ATOMIC_RELAXED, __HIP_MEMORY_SCOPE_WORKGROUP); } while (0)
#define LC_WAIT(i) do { while (__hip_atomic_load(lc + (i), __ATOMIC_RELAXED, __HIP_MEMORY_SCOPE_WORKGROUP) < 4u) __builtin_amdgcn_s_sleep(1); asm volatile("" ::: "memory"); } while (0)
  if (w >= 4) {
#pragma unroll
    for (int c = 5; c < 9; ++c) issue_wd4(c, w - 4);
    LC_WAIT(3);
#pragma unroll
    for (int c = 9; c < 13; ++c) issue_wd4(c, w - 4);
    WAITV(16);
    LC_ADD(4);
    WAITV(0);
    LC_ADD(9);
    LC_WAIT(5);
    issue_wd4(13, w - 4);
    WAITV(0);
    LC_ADD(6);
  } else {
    const int ct = c8;
    unsigned wl[2], wh[2], wx[2];
#pragma unroll
    for (int T = 0; T < 2; ++T) {
      wl[T] = wt[T] + kh * 8192; wh[T] = wl[T] + 65536; wx[T] = wl[T] + 131072;
      asm volatile("" : "+v"(wl[T]), "+v"(wh[T]), "+v"(wx[T]));
    }
    auto wdfrag = [&](int sx, int T) {
      const int sl = wd_slot(sx >> 2), o = ((sx >> 1) & 1) * 4096 + (sx & 1) * 2048;
      return sl >= 131072 ? tr_frag((LAS unsigned char*)(size_t)wx[T] + o)
           : sl >= 65536 ? tr_frag((LAS unsigned char*)(size_t)wh[T] + (sl - 65536) + o) : tr_frag((LAS unsigned char*)(size_t)wl[T] + sl + o);
    };
    bf16x8 wr[16][2];
#pragma unroll
    for (int sx = 0; sx < 16; ++sx)
#pragma unroll
      for (int T = 0; T < 2; ++T) wr[sx][T] = wdfrag(sx, T);
    LC_ADD(3);
    for (unsigned spins = 0;;) {
      const bool ok = __hip_atomic_load(fl + (FL_C3 + bx * 128) / 4, RLX_AGENT) >= 28u;
      if (__all(ok)) break;
      if (poll_fail(spins, fl, lane)) break;
    }
    __builtin_amdgcn_fence(__ATOMIC_ACQUIRE, "wavefront");
    if (w == 0 && bi == 31 && lane == (bx & 1)) __hip_atomic_store(fl + (FL_E + bx * 256) / 4, tagv, RLX_AGENT);
    STAMP(31);
    constexpr int AW = 10;
    bf16x8 a[AW];
    const unsigned asrc = WS_ACT + ((4 * rt + 2 * mh + (tk >> 4)) * 56 + kh * 28) * 1024 + (hf * 16 + (tk & 15)) * 16;
    auto lda = [&](int sx) { const int c = sx >> 2, pc_ = c + r4 >= 14 ? c + r4 - 14 : c + r4;
      a[sx % AW] = __builtin_bit_cast(bf16x8, __builtin_amdgcn_raw_buffer_load_b128(rws, asrc + ((sx >> 1) & 1) * 1024 + (sx & 1) * 512, pc_ * 2048, AUX_SC1)); };
#pragma unroll
    for (int sx = 0; sx < AW; ++sx) lda(sx);
    f32x16 acc[2];
#pragma unroll
    for (int i = 0; i < 16; ++i) { acc[0][i] = 0.f; acc[1][i] = 0.f; }
    bf16x8 wf[2][2];
    SB();
    static_for<0, 56>([&](auto SS) {
      constexpr int sx = decltype(SS)::value;
      if (sx == 19) LC_WAIT(4);
      if (sx == 35) LC_WAIT(9);
      if (sx == 20) LC_ADD(5);
      if (sx == 51) LC_WAIT(6);
      if (sx + 1 >= 16 && sx + 1 < 56) { wf[(sx + 1) & 1][0] = wdfrag(sx + 1, 0); wf[(sx + 1) & 1][1] = wdfrag(sx + 1, 1); }
      acc[0] = __builtin_amdgcn_mfma_f32_32x32x16_bf16(sx < 16 ? wr[sx & 15][0] : wf[sx & 1][0], a[sx % AW], acc[0], 0, 0, 0);
      acc[1] = __builtin_amdgcn_mfma_f32_32x32x16_bf16(sx < 16 ? wr[sx & 15][1] : wf[sx & 1][1], a[sx % AW], acc[1], 0, 0, 0);
      if (sx + AW < 56) lda(sx + AW);
      SB();
    });
    STAMP(33);
    LAS f32x4* red = (LAS f32x4*)(L + SM - 16384);
    LC_ADD(7);
    LC_WAIT(7);
#pragma unroll
    for (int j = 0; j < 4; ++j) {
      const f32x16& o = acc[kh ^ 1];
      red[(((mh * 2 + kh) * 4) + j) * 64 + lane] = f32x4{o[4 * j], o[4 * j + 1], o[4 * j + 2], o[4 * j + 3]};
    }
    LC_ADD(8);
    LC_WAIT(8);
    unsigned op[8];
#pragma unroll
    for (int j = 0; j < 4; ++j) {
      const f32x4 o = red[(((mh * 2 + (kh ^ 1)) * 4) + j) * 64 + lane];
      const f32x16& m = acc[kh];
      const unsigned y01 = pack2(m[4 * j] + o[0], m[4 * j + 1] + o[1]), y23 = pack2(m[4 * j + 2] + o[2], m[4 * j + 3] + o[3]);
      op[2 * j] = pack2(bflo(hp[2 * j]) + bflo(y01), bfhi(hp[2 * j]) + bfhi(y01));
      op[2 * j + 1] = pack2(bflo(hp[2 * j + 1]) + bflo(y23), bfhi(hp[2 * j + 1]) + bfhi(y23));
    }
    unsigned short* orow = p.out + (size_t)(64 * rt + 32 * mh + tk) * D_ + 64 * ct + 32 * kh + 8 * hf;
#pragma unroll
    for (int jj = 0; jj < 2; ++jj) {
      const auto a0 = __builtin_amdgcn_permlane32_swap(op[4 * jj], op[4 * jj + 2], false, false);
      const auto a1 = __builtin_amdgcn_permlane32_swap(op[4 * jj + 1], op[4 * jj + 3], false, false);
      const v4u o = {a0[0], a1[0], a0[1], a1[1]};
      *(GAS v4u*)(orow + 16 * jj) = o;
    }
    STAMP(34);
  }
  STAMP_END();
}

#if defined(__HIP_DEVICE_COMPILE__)
#pragma clang attribute pop
#endif
extern "C" void kernel_launch(void* const* d_in, const int* in_sizes, int n_in, void* d_out, int out_size, void* d_ws, size_t ws_size, hipStream_t stream) {
  Params p;
  p.x = (const unsigned short*)d_in[0];
  p.wqkv = (const unsigned short*)d_in[1];
  p.wo = (const unsigned short*)d_in[2];
  p.wg = (const unsigned short*)d_in[4];
  p.wu = (const unsigned short*)d_in[5];
  p.wd = (const unsigned short*)d_in[6];
  p.out = (unsigned short*)d_out;
  p.ws = (unsigned char*)d_ws;
  constexpr int LDS = 160 * 1024;
  (void)hipFuncSetAttribute((const void*)k_fused, hipFuncAttributeMaxDynamicSharedMemorySize, LDS);
  k_fused<<<256, 512, LDS, stream>>>(p);
}
```

```cpp
#define HD 8
#include <hip/hip_runtime.h>
#include <hip/hip_bf16.h>

#define GAS __attribute__((address_space(1)))
#define LAS __attribute__((address_space(3)))
typedef short bf16x8 __attribute__((ext_vector_type(8)));
typedef short bf16x4 __attribute__((ext_vector_type(4)));
typedef __bf16 bf2 __attribute__((ext_vector_type(2)));
typedef float f32x4 __attribute__((ext_vector_type(4)));
typedef float f32x2 __attribute__((ext_vector_type(2)));
typedef float f32x16 __attribute__((ext_vector_type(16)));
typedef unsigned v4u __attribute__((ext_vector_type(4)));
typedef unsigned v2u __attribute__((ext_vector_type(2)));
#define DI __device__ __forceinline__

constexpr int S_ = 2048, D_ = 512, F_ = 1792;
constexpr int AUX_SC1 = 16;
constexpr unsigned RSRC3 = 0x00020000u;
constexpr unsigned WS_ATT = 0u;
constexpr unsigned WS_HF = 2u << 20;
constexpr unsigned WS_ACT = 4u << 20;
constexpr unsigned WS_ST = 12u << 20;
constexpr unsigned WS_FL = 13u << 20;
constexpr unsigned FL_E = 0, FL_F1 = 4096, FL_C2 = 16384, FL_C3 = 24576, FL_TMO = 28672;
constexpr unsigned WS_BYTES = 14u << 20;
typedef GAS unsigned gu32;
#define RLX_AGENT __ATOMIC_RELAXED, __HIP_MEMORY_SCOPE_AGENT
constexpr unsigned SPIN_CAP = 1u << 18;

struct Params {
  const unsigned short* x;
  const unsigned short* wqkv;
  const unsigned short* wo;
  const unsigned short* wg;
  const unsigned short* wu;
  const unsigned short* wd;
  unsigned short* out;
  unsigned char* ws;
};

#if defined(__HIP_DEVICE_COMPILE__)
#pragma clang attribute push (__attribute__((target("no-packed-fp32-ops"))), apply_to = function)
#endif
template <int V> struct IC { static constexpr int value = V; };
template <int A, int B, class F> __device__ __forceinline__ void static_for(F&& f) { if constexpr (A < B) { f(IC<A>{}); static_for<A + 1, B>(f); } }
DI float asf(unsigned u) { return __builtin_bit_cast(float, u); }
DI unsigned asu(float f) { return __builtin_bit_cast(unsigned, f); }
DI f32x4 mfma32(bf16x8 a, bf16x8 b, f32x4 c) { return __builtin_amdgcn_mfma_f32_16x16x32_bf16(a, b, c, 0, 0, 0); }
DI f32x4 mfma16(bf16x4 a, bf16x4 b, f32x4 c) { return __builtin_amdgcn_mfma_f32_16x16x16bf16_1k(a, b, c, 0, 0, 0); }
DI unsigned pack2(float lo, float hi) { bf2 p; p[0] = (__bf16)lo; p[1] = (__bf16)hi; return __builtin_bit_cast(unsigned, p); }
DI float bflo(unsigned u) { return __builtin_bit_cast(float, u << 16); }
DI float bfhi(unsigned u) { return __builtin_bit_cast(float, u & 0xffff0000u); }
DI unsigned ones2() { unsigned o = 0x3F803F80u; asm volatile("" : "+v"(o)); return o; }
DI float dot2(unsigned a, unsigned b, float c) { return __builtin_amdgcn_fdot2_f32_bf16(__builtin_bit_cast(bf2, a), __builtin_bit_cast(bf2, b), c, false); }
DI __amdgpu_buffer_rsrc_t mkrs(const void* p, unsigned bytes) { return __builtin_amdgcn_make_buffer_rsrc((void*)p, 0, bytes, RSRC3); }
DI void dma16(__amdgpu_buffer_rsrc_t rs, unsigned dst, unsigned voff, unsigned soff) {
  asm volatile("s_mov_b32 m0, %0\n\ts_nop 0\n\tbuffer_load_dwordx4 %1, %2, %3 offen lds"
               :: "s"(dst), "v"(voff), "s"(rs), "s"(soff) : "m0", "memory");
}
template <int SS> DI void dma16x4(__amdgpu_buffer_rsrc_t rs, unsigned dst, unsigned v0, unsigned v1, unsigned soff) {
  unsigned t;
  asm volatile("s_mov_b32 m0, %1\n\ts_add_u32 %0, %5, %6\n\t"
               "buffer_load_dwordx4 %2, %4, %5 offen lds\n\t"
               "buffer_load_dwordx4 %3, %4, %0 offen offset:1024 lds\n\ts_add_u32 %0, %0, %6\n\t"
               "buffer_load_dwordx4 %2, %4, %0 offen offset:2048 lds\n\ts_add_u32 %0, %0, %6\n\t"
               "buffer_load_dwordx4 %3, %4, %0 offen offset:3072 lds"
               : "=&s"(t) : "s"(dst), "v"(v0), "v"(v1), "s"(rs), "s"(soff), "n"(SS - 1024) : "m0", "scc", "memory");
}
template <int LS> DI void dma16x3(__amdgpu_buffer_rsrc_t rs, unsigned dst, unsigned v, unsigned soff) {
  asm volatile("s_mov_b32 m0, %0\n\ts_nop 0\n\t"
               "buffer_load_dwordx4 %1, %2, %3 offen lds\n\ts_add_u32 m0, m0, %4\n\ts_nop 0\n\t"
               "buffer_load_dwordx4 %1, %2, %3 offen offset:1024 lds\n\ts_add_u32 m0, m0, %4\n\ts_nop 0\n\t"
               "buffer_load_dwordx4 %1, %2, %3 offen offset:2048 lds"
               :: "s"(dst), "v"(v), "s"(rs), "s"(soff), "n"(LS - 1024) : "m0", "scc", "memory");
}
DI bf16x8 lds_frag(LAS unsigned char* p) { return *(LAS bf16x8*)p; }
DI bf16x8 tr_frag(LAS unsigned char* p) {
  bf16x4 lo = __builtin_amdgcn_ds_read_tr16_b64_v4i16((LAS bf16x4*)p);
  bf16x4 hi = __builtin_amdgcn_ds_read_tr16_b64_v4i16((LAS bf16x4*)(p + 512));
  return __builtin_shufflevector(lo, hi, 0, 1, 2, 3, 4, 5, 6, 7);
}
DI void swap16(float v, float& a, float& b) {
  const unsigned u = asu(v);
  auto r = __builtin_amdgcn_permlane16_swap(u, u, false, false);
  a = asf(r[0]); b = asf(r[1]);
}
DI void swap32(float v, float& a, float& b) {
  const unsigned u = asu(v);
  auto r = __builtin_amdgcn_permlane32_swap(u, u, false, false);
  a = asf(r[0]); b = asf(r[1]);
}
DI float xsum_g(float v) {
  const f32x4 z = {0.f, 0.f, 0.f, 0.f};
  asm volatile("s_nop 3" : "+v"(v));
  return __builtin_amdgcn_mfma_f32_16x16x4f32(1.0f, v, z, 0, 0, 0)[0];
}
DI float xmax_g(float v) {
  float a, b;
  swap16(v, a, b); v = fmaxf(a, b);
  swap32(v, a, b); return fmaxf(a, b);
}
#define WAITV(n) asm volatile("s_waitcnt vmcnt(" #n ")" ::: "memory")
#define WAITL0() asm volatile("s_waitcnt lgkmcnt(0)" ::: "memory")
#define BAR() asm volatile("s_barrier" ::: "memory")
#define SB() __builtin_amdgcn_sched_barrier(0)
#define SG(mask, n) __builtin_amdgcn_sched_group_barrier(mask, n, 0)
#define M_MFMA 0x8
#define M_DSR 0x100
#define M_DSW 0x200
#define M_VALU 0x2
#define M_TRANS 0x400
#define M_VMEMR 0x20
constexpr float LN_EPS = 1e-5f;
#define STAMP_DECL do {} while (0)
#define STAMP(i) do {} while (0)
#define STAMP_END() do {} while (0)

DI unsigned wtr_lane(int lane, int nt) {
  const int g = lane >> 4, q = (lane & 15) >> 2, p = lane & 3;
  const int sg = (q >> 1) | ((g & 1) << 1);
  return g * 1024 + q * 128 + ((nt ^ sg) << 5) + p * 8;
}
DI unsigned wdma32_lane(int lane, unsigned pitch) {
  const int r8 = lane >> 3, pos = lane & 7;
  return r8 * pitch + ((pos ^ (((r8 >> 1) & 1) << 2)) << 4);
}
DI unsigned wdma_lane(int lane, unsigned pitch, int odd) {
  const int r8 = lane >> 3, pos = lane & 7;
  return r8 * pitch + ((pos ^ (((r8 >> 1) & 1) << 1) ^ (odd << 2)) << 4);
}

constexpr int P1_X = 0;
constexpr int P1_W = 81920;
constexpr int SM = 155648;
constexpr int P1_PART = SM;
constexpr int P1_CS = SM + 2560;
constexpr int P1_FIN = SM + 4096;
constexpr int LC = SM + 7168;
constexpr int P3_CS = SM;
constexpr int WO_IMG = 65536;
constexpr int RED = 131072;
constexpr int GU_A = 0;
constexpr int GU_B = 65536;
DI constexpr int wd_slot(int c) { return (c % 9) == 0 ? 131072 : ((c % 9) - 1) * 16384; }

DI bool poll_fail(unsigned& spins, gu32* fl, int lane) {
  if (++spins > SPIN_CAP) { if (lane == 0) __hip_atomic_store(fl + FL_TMO / 4, 1u, RLX_AGENT); return true; }
  return false;
}

__global__ __launch_bounds__(512, 2) void k_fused(Params p) {
  extern __shared__ __attribute__((aligned(16))) unsigned char smem[];
  LAS unsigned char* L = (LAS unsigned char*)smem;
  const int tid = threadIdx.x, lane = tid & 63, w = __builtin_amdgcn_readfirstlane(tid >> 6);
  const int bx = blockIdx.x & 7, bi = blockIdx.x >> 3, rt = 4 * bx + (bi >> 3), c8 = bi & 7;
  const int pr = bx >> 1, pc = bx & 1;
  const int r = lane & 15, g = lane >> 4;
  const __amdgpu_buffer_rsrc_t rws = mkrs(p.ws, WS_BYTES);
  gu32* fl = (gu32*)(p.ws + WS_FL);
  const f32x4 z4 = {0.f, 0.f, 0.f, 0.f};
  const bf16x8 ones = {0x3F80, 0x3F80, 0x3F80, 0x3F80, 0x3F80, 0x3F80, 0x3F80, 0x3F80};
  const unsigned o2 = ones2();
  const int rt1 = 8 * pr + (bi >> 2), hg = 4 * pc + (bi & 3);
  const bool has3 = bi < 28;
  const int pn = 2 * pr + (bi >= 14 ? 1 : 0), fs = 14 * pc + (bi >= 14 ? bi - 14 : bi);
  const int r2 = (c8 + 2 * (rt & 3)) & 7, r3 = fs & 7, fl3 = bi >= 14 ? 8 : 0, r4 = (2 * c8 + 3 * (rt & 3)) % 14;
  if (tid < 16) ((LAS unsigned*)(L + LC))[tid] = 0u;
  STAMP_DECL;
  STAMP(0);
  const unsigned tagv = __hip_atomic_load(fl + (FL_E + (2 * pr + (lane & 1)) * 256) / 4, RLX_AGENT) + 1u;
  if (w == 0) {
    if (hg == 0 && lane == 0) {
      __hip_atomic_store(fl + (FL_C2 + rt1 * 128) / 4, 0u, RLX_AGENT);
      if ((rt1 & 3) == 0) __hip_atomic_store(fl + (FL_C3 + (rt1 >> 2) * 128) / 4, 0u, RLX_AGENT);
    }
  }
  {
    const int hh = w & 3, rh = w >> 2, rot = (bi + (bi >> 2) + 2 * pr) & 7;
    const __amdgpu_buffer_rsrc_t rx = mkrs(p.x, S_ * D_ * 2), rw = mkrs(p.wqkv, D_ * 3 * D_ * 2);
    const int r8 = lane >> 3, pos = lane & 7;
    const unsigned xv0 = r8 * 1024 + ((pos ^ (r8 >> 1)) << 4), xv1 = xv0 ^ 64;
    auto issue_x = [&](int half) {
      const int c = 4 * half + (w >> 1);
#pragma unroll
      for (int i = 0; i < 5; ++i) {
        const int P = 5 * (w & 1) + i;
        int row = 64 * rt1 - 16 + 8 * P;
        row = row < 0 ? 0 : row;
        dma16(rx, P1_X + (c * 10 + P) * 1024, (P & 1) ? xv1 : xv0, row * 1024 + ((c + rot) & 7) * 128);
      }
    };
    const unsigned wv = wdma_lane(lane, 3072, w & 1);
    auto issue_w = [&](int c) {
      dma16x3<8192>(rw, P1_W + (c % 3) * 24576 + w * 1024, wv, (((c + rot) & 7) * 64 + 8 * w) * 3072 + hg * 128);
    };
    issue_x(0); issue_w(0); issue_w(1);
    STAMP(1);
    f32x4 aq[2] = {z4, z4}, ak[3] = {z4, z4, z4}, av[3] = {z4, z4, z4}, aqh = z4;
    float s1 = 0.f, s2 = 0.f;
    const unsigned xlane = (r >> 3) * 1024 + (r & 7) * 128 + ((g ^ ((r >> 1) & 7)) << 4);
    unsigned xl0 = xlane + 2 * rh * 2048, xl1 = xl0 ^ 64;
    const int stile = rh == 0 ? (hh < 3 ? hh : 0) : (hh < 2 ? 3 + hh : 3);
    unsigned xs0 = xlane + stile * 2048, xs1 = xs0 ^ 64;
    unsigned wlA = wtr_lane(lane, hh) + P1_W, wlB = wlA + 49152;
    unsigned xh0 = xl0 + 40960, xh1 = xl1 + 40960, xsh0 = xs0 + 40960, xsh1 = xs1 + 40960;
    asm volatile("" : "+v"(xl0), "+v"(xl1), "+v"(xs0), "+v"(xs1), "+v"(wlA), "+v"(wlB), "+v"(xh0), "+v"(xh1), "+v"(xsh0), "+v"(xsh1));
    bf16x8 fx[2][4];
    bf16x4 fw[2][3][2];
    auto rd1 = [&](int K, int j) {
      const int c = K >> 1, sx = K & 1, bf = K & 1;
      if (j < 6) {
        const int part = j >> 1, h = j & 1;
        const unsigned base = (c % 3) == 2 ? wlB : wlA;
        const int off = ((c % 3) == 2 ? 0 : (c % 3) * 24576) + part * 8192 + sx * 4096 + h * 512;
        fw[bf][part][h] = __builtin_amdgcn_ds_read_tr16_b64_v4i16((LAS bf16x4*)((LAS unsigned char*)(size_t)base + off));
      } else if (j < 9) {
        const int t = j == 6 ? 1 : j == 7 ? 2 : 0;
        const unsigned base = c < 4 ? (sx ? xl1 : xl0) : (sx ? xh1 : xh0);
        fx[bf][t] = lds_frag((LAS unsigned char*)(size_t)base + (c & 3) * 10240 + t * 2048);
      } else {
        const unsigned base = c < 4 ? (sx ? xs1 : xs0) : (sx ? xsh1 : xsh0);
        fx[bf][3] = lds_frag((LAS unsigned char*)(size_t)base + (c & 3) * 10240);
      }
    };
    constexpr int RORD[10] = {0, 1, 6, 7, 2, 3, 8, 4, 5, 9};
    auto wfrag = [&](int bf, int part) { return __builtin_shufflevector(fw[bf][part][0], fw[bf][part][1], 0, 1, 2, 3, 4, 5, 6, 7); };
    auto mm1 = [&](int K, int j) {
      const int bf = K & 1;
      if (j == 0) aq[0] = mfma32(wfrag(bf, 0), fx[bf][1], aq[0]);
      else if (j == 1) aq[1] = mfma32(wfrag(bf, 0), fx[bf][2], aq[1]);
      else if (j == 2) ak[1] = mfma32(wfrag(bf, 1), fx[bf][1], ak[1]);
      else if (j == 3) ak[2] = mfma32(wfrag(bf, 1), fx[bf][2], ak[2]);
      else if (j == 4) ak[0] = mfma32(wfrag(bf, 1), fx[bf][0], ak[0]);
      else if (j == 5) av[0] = mfma32(fx[bf][0], wfrag(bf, 2), av[0]);
      else if (j == 6) av[1] = mfma32(fx[bf][1], wfrag(bf, 2), av[1]);
      else av[2] = mfma32(fx[bf][2], wfrag(bf, 2), av[2]);
    };
    auto ones_row = [&](int half) {
      if ((w & 1) == 0 && lane < 8) *(LAS v4u*)(L + P1_X + (4 * half + (w >> 1)) * 10240 + lane * 16) = v4u{0x3F803F80u, 0x3F803F80u, 0x3F803F80u, 0x3F803F80u};
    };
    f32x4 b0, b1;
#pragma unroll
    for (int i = 0; i < 4; ++i) {
      const int d0 = 16 + r - (4 * g + i), d1 = r - (4 * g + i);
      b0[i] = (d0 <= 14 && !(d0 & 1)) ? 0.f : -1.0e30f;
      b1[i] = (d1 >= 0 && d1 <= 14 && !(d1 & 1)) ? 0.f : -1.0e30f;
    }
    asm volatile("" : "+v"(b0), "+v"(b1));
    WAITV(3);
    ones_row(0);
    WAITL0();
    BAR();
    STAMP(2);
    issue_w(2);
    v4u wu_[3];
    {
      const unsigned b = blockIdx.x;
      const GAS v4u* q0 = w < 7 ? (const GAS v4u*)p.wg + b * 448 + w * 64 + lane : (const GAS v4u*)p.wo + b * 128 + lane;
      const GAS v4u* q1 = w < 7 ? (const GAS v4u*)p.wu + b * 448 + w * 64 + lane : (const GAS v4u*)p.wo + b * 128 + 64 + lane;
      const GAS v4u* q2 = (const GAS v4u*)p.wd + b * 448 + (w < 7 ? w : 6) * 64 + lane;
      asm volatile("global_load_dwordx4 %0, %3, off\n\tglobal_load_dwordx4 %1, %4, off\n\tglobal_load_dwordx4 %2, %5, off"
                   : "=&v"(wu_[0]), "=&v"(wu_[1]), "=&v"(wu_[2]) : "v"(q0), "v"(q1), "v"(q2) : "memory");
    }
#pragma unroll
    for (int j = 0; j < 10; ++j) rd1(0, RORD[j]);
    SB();
    static_for<0, 16>([&](auto KK) {
      constexpr int K = decltype(KK)::value, c = K >> 1;
      if constexpr ((K & 1) == 1 && c < 7) {
        if (c == 0) WAITV(6);
        else if (c == 1) WAITV(11);
        else if (c == 2) WAITV(8);
        else if (c == 6) WAITV(0);
        else WAITV(3);
        if (c == 2) asm volatile("" :: "v"(wu_[0]), "v"(wu_[1]), "v"(wu_[2]));
        if (c == 3) ones_row(1);
        WAITL0();
        BAR();
        STAMP(3 + c);
        if (c + 3 < 8) issue_w(c + 3);
        if (c == 0) issue_x(1);
        SB();
      }
      static_for<0, 8>([&](auto JJ) {
        constexpr int j = decltype(JJ)::value;
        mm1(K, j);
        if (K + 1 < 16) { rd1(K + 1, RORD[j]); if (j >= 6) rd1(K + 1, RORD[j + 2]); }
        { const v4u u = __builtin_bit_cast(v4u, fx[K & 1][3]);
          if (j & 1) s2 = dot2(u[j >> 1], u[j >> 1], s2); else s1 = dot2(u[j >> 1], o2, s1); }
        SB();
      });
      if (rh == 0) aqh = mfma32(wfrag(K & 1, 0), fx[K & 1][0], aqh);
      SB();
    });
    STAMP(10);
    LAS f32x2* fin = (LAS f32x2*)(L + P1_FIN);
    LAS float* csx = (LAS float*)(L + P1_CS);
    {
      const float a = xsum_g(s1), b = xsum_g(s2);
      const float mean = a * (1.f / D_);
      const float rstd = __builtin_amdgcn_rsqf(fmaxf(b * (1.f / D_) - mean * mean, 0.f) + LN_EPS);
      if (lane < 16 && (rh == 0 ? hh < 3 : hh < 2)) fin[stile * 16 + lane] = f32x2{mean, rstd};
      if (rh == 0) {
        if (r == 0) { *(LAS f32x4*)&csx[hh * 48 + 4 * g] = aqh; *(LAS f32x4*)&csx[hh * 48 + 16 + 4 * g] = ak[0]; }
        if (lane < 16) csx[hh * 48 + 32 + lane] = av[0][0];
      }
    }
    WAITL0();
    BAR();
    {
      const __amdgpu_buffer_rsrc_t rwo = mkrs(p.wo, D_ * D_ * 2);
      const unsigned a0 = wdma32_lane(lane, 1024), a1 = a0;
      dma16x4<8192>(rwo, WO_IMG + w * 8192, a0, a1, (((2 * w) & 8) | ((2 * w + r2) & 7)) * 32768 + c8 * 128);
      dma16x4<8192>(rwo, WO_IMG + w * 8192 + 4096, a0, a1, (((2 * w) & 8) | ((2 * w + 1 + r2) & 7)) * 32768 + c8 * 128);
    }
    const f32x4 cq = *(LAS f32x4*)&csx[hh * 48 + 4 * g], ck = *(LAS f32x4*)&csx[hh * 48 + 16 + 4 * g];
    const float cv0 = csx[hh * 48 + 32 + r];
    STAMP(11);
    bf16x4 Qp[2], Kp[3], Vp[3];
#pragma unroll
    for (int t = 0; t < 3; ++t) {
      const f32x2 st = fin[(2 * rh + t) * 16 + r];
      const f32x4 s01 = *(LAS f32x4*)&fin[(2 * rh + t) * 16 + 4 * g], s23 = *(LAS f32x4*)&fin[(2 * rh + t) * 16 + 4 * g + 2];
      const float nm = -st[0], rs = st[1];
      v2u u;
      u[0] = pack2(fmaf(nm, ck[0], ak[t][0]) * rs, fmaf(nm, ck[1], ak[t][1]) * rs);
      u[1] = pack2(fmaf(nm, ck[2], ak[t][2]) * rs, fmaf(nm, ck[3], ak[t][3]) * rs);
      Kp[t] = __builtin_bit_cast(bf16x4, u);
      if (t >= 1) {
        u[0] = pack2(fmaf(nm, cq[0], aq[t - 1][0]) * rs, fmaf(nm, cq[1], aq[t - 1][1]) * rs);
        u[1] = pack2(fmaf(nm, cq[2], aq[t - 1][2]) * rs, fmaf(nm, cq[3], aq[t - 1][3]) * rs);
        Qp[t - 1] = __builtin_bit_cast(bf16x4, u);
      }
      u[0] = pack2(fmaf(-s01[0], cv0, av[t][0]) * s01[1], fmaf(-s01[2], cv0, av[t][1]) * s01[3]);
      u[1] = pack2(fmaf(-s23[0], cv0, av[t][2]) * s23[1], fmaf(-s23[2], cv0, av[t][3]) * s23[3]);
      Vp[t] = __builtin_bit_cast(bf16x4, u);
    }
    v2u O[2];
#pragma unroll
    for (int qt = 0; qt < 2; ++qt) {
      f32x4 bb = b0;
      if (qt == 0 && rt1 == 0 && rh == 0) bb = f32x4{-1.0e30f, -1.0e30f, -1.0e30f, -1.0e30f};
      const f32x4 S0 = mfma16(Kp[qt], Qp[qt], bb), S1 = mfma16(Kp[qt + 1], Qp[qt], b1);
      float m = fmaxf(fmaxf(fmaxf(S0[0], S0[1]), fmaxf(S0[2], S0[3])), fmaxf(fmaxf(S1[0], S1[1]), fmaxf(S1[2], S1[3])));
      m = xmax_g(m);
      constexpr float SC = 0.25f * 1.4426950408889634f;
      const float ms = -m * SC;
      float v[8];
#pragma unroll
      for (int i = 0; i < 4; ++i) { v[i] = __builtin_amdgcn_exp2f(fmaf(S0[i], SC, ms)); v[4 + i] = __builtin_amdgcn_exp2f(fmaf(S1[i], SC, ms)); }
      float sum = ((v[0] + v[1]) + (v[2] + v[3])) + ((v[4] + v[5]) + (v[6] + v[7]));
      sum = xsum_g(sum);
      const float inv = __builtin_amdgcn_rcpf(sum);
      v2u p0, p1;
      p0[0] = pack2(v[0] * inv, v[1] * inv); p0[1] = pack2(v[2] * inv, v[3] * inv);
      p1[0] = pack2(v[4] * inv, v[5] * inv); p1[1] = pack2(v[6] * inv, v[7] * inv);
      f32x4 o = mfma16(Vp[qt], __builtin_bit_cast(bf16x4, p0), z4);
      o = mfma16(Vp[qt + 1], __builtin_bit_cast(bf16x4, p1), o);
      O[qt][0] = pack2(o[0], o[1]); O[qt][1] = pack2(o[2], o[3]);
    }
    {
      const auto a = __builtin_amdgcn_permlane16_swap(O[0][0], O[1][0], false, false);
      const auto b = __builtin_amdgcn_permlane16_swap(O[0][1], O[1][1], false, false);
      const v4u o = {a[0], b[0], a[1], b[1]};
      const int head = 4 * hg + hh;
      const unsigned off = WS_ATT + ((4 * rt1 + 2 * rh + (g & 1)) * 16 + (head >> 1)) * 1024 + ((2 * (head & 1) + (g >> 1)) * 16 + r) * 16;
      __builtin_amdgcn_raw_buffer_store_b128(o, rws, off, 0, AUX_SC1);
    }
    STAMP(12);
    WAITV(0);
    BAR();
    if (tid == ((rt1 >> 2) & 1)) __hip_atomic_store(fl + (FL_F1 + rt1 * 128) / 4 + hg, tagv, RLX_AGENT);
    STAMP(13);
  }
  const __amdgpu_buffer_rsrc_t rg = mkrs(p.wg, D_ * F_ * 2), ru = mkrs(p.wu, D_ * F_ * 2);
  const unsigned gl0 = wdma_lane(lane, F_ * 2, 0), gl1 = gl0 ^ 64;
#define THR() asm volatile("s_waitcnt vmcnt(%0)" :: "n"(HD) : "memory")
  auto issue_gu2 = [&](int half, int ks, int dst) {
    const unsigned so = ((((8 * half) ^ fl3) | ((ks + r3) & 7)) * 32) * (F_ * 2) + fs * 128;
    dma16x4<8 * F_ * 2>(rg, dst + ks * 8192, gl0, gl1, so);
    THR();
    dma16x4<8 * F_ * 2>(ru, dst + ks * 8192 + 4096, gl1, gl0, so);
    THR();
  };
  auto issue_gu = [&](int half, int dst) {
    const unsigned so = ((((8 * half) ^ fl3) | ((w + r3) & 7)) * 32) * (F_ * 2) + fs * 128;
    dma16x4<8 * F_ * 2>(rg, dst + w * 8192, gl0, gl1, so);
    dma16x4<8 * F_ * 2>(ru, dst + w * 8192 + 4096, gl1, gl0, so);
  };
  const __amdgpu_buffer_rsrc_t rwd = mkrs(p.wd, F_ * D_ * 2);
  const unsigned dl = wdma32_lane(lane, 1024);
  auto issue_wd4 = [&](int c, int qd) {
    const int pc_ = c + r4 >= 14 ? c + r4 - 14 : c + r4;
    dma16x4<8192>(rwd, wd_slot(c) + qd * 4096, dl, dl, (((qd >> 1) * 28 + 2 * pc_ + (qd & 1)) * 32) * 1024 + c8 * 128);
    THR();
  };
  auto issue_wd = [&](int c) {
    const int i = w * 2, half = i >> 3, ksl = (i >> 2) & 1, pk = i & 3;
    const int pc_ = c + r4 >= 14 ? c + r4 - 14 : c + r4;
    const unsigned so = ((half * 28 + 2 * pc_ + ksl) * 32 + 8 * pk) * 1024 + c8 * 128;
    dma16(rwd, wd_slot(c) + i * 1024, dl, so);
    dma16(rwd, wd_slot(c) + i * 1024 + 1024, dl, so + 8192);
  };
  unsigned hp[8];
  const int tk = lane & 31, hf = lane >> 5, mh = w & 1, kh = (w >> 1) & 1;
  unsigned wt[2];
  {
    const int q = (lane & 15) >> 2, pp = lane & 3, bb = (lane >> 4) & 1;
#pragma unroll
    for (int T = 0; T < 2; ++T) wt[T] = hf * 1024 + q * 128 + (((4 * T + 2 * bb + (pp >> 1)) ^ ((q >> 1) << 2)) << 4) + (pp & 1) * 8;
  }
  LAS unsigned* lc = (LAS unsigned*)(L + LC);
  if (w >= 4) {
    if (has3) {
      issue_gu2(0, 2 * (w - 4), GU_A); issue_gu2(0, 2 * (w - 4) + 1, GU_A);
      while (__hip_atomic_load(lc, __ATOMIC_RELAXED, __HIP_MEMORY_SCOPE_WORKGROUP) < 4u) __builtin_amdgcn_s_sleep(1);
      issue_gu2(1, 2 * (w - 4), GU_B); issue_gu2(1, 2 * (w - 4) + 1, GU_B);
    } else {
      issue_wd4(1, w - 4); issue_wd4(2, w - 4); issue_wd4(3, w - 4); issue_wd4(4, w - 4);
    }
  } else {
    const int ct = c8;
    const int row = 64 * rt + 32 * mh + tk;
    v2u xr[4];
#pragma unroll
    for (int j = 0; j < 4; ++j) xr[j] = *(const GAS v2u*)(p.x + (size_t)row * D_ + 64 * ct + 32 * kh + 8 * j + 4 * hf);
    bf16x8 wf[16][2];
    {
      unsigned wb[2] = {wt[0] + WO_IMG + kh * 32768, wt[1] + WO_IMG + kh * 32768};
      asm volatile("" : "+v"(wb[0]), "+v"(wb[1]));
#pragma unroll
      for (int sx = 0; sx < 16; ++sx)
#pragma unroll
        for (int T = 0; T < 2; ++T) wf[sx][T] = tr_frag((LAS unsigned char*)(size_t)wb[T] + (sx >> 1) * 4096 + (sx & 1) * 2048);
    }
    WAITL0();
    if (lane == 0) __hip_atomic_fetch_add(lc, 1u, __ATOMIC_RELAXED, __HIP_MEMORY_SCOPE_WORKGROUP);
    {
      const int frt = lane < 8 ? rt : lane < 12 ? 4 * bx + (lane & 3) : 4 * pn + (lane & 3);
      const unsigned idx = (FL_F1 + frt * 128) / 4 + (lane < 8 ? lane : 0);
      const unsigned tg = __shfl(tagv, (frt >> 2) & 1);
      for (unsigned spins = 0;;) {
        const bool ok = lane >= 16 || __hip_atomic_load(fl + idx, RLX_AGENT) == tg;
        if (__all(ok)) break;
        if (poll_fail(spins, fl, lane)) break;
      }
      __builtin_amdgcn_fence(__ATOMIC_ACQUIRE, "wavefront");
    }
    STAMP(14);
    bf16x8 a[16];
    {
      const unsigned av_ = WS_ATT + ((4 * rt + 2 * mh + (tk >> 4)) * 16 + kh * 8) * 1024 + (hf * 16 + (tk & 15)) * 16;
#pragma unroll
      for (int sx = 0; sx < 16; ++sx)
        a[sx] = __builtin_bit_cast(bf16x8, __builtin_amdgcn_raw_buffer_load_b128(rws, av_ + (sx & 1) * 512, (((sx >> 1) + r2) & 7) * 1024, AUX_SC1));
    }
    SB();
    f32x16 acc[2];
#pragma unroll
    for (int i = 0; i < 16; ++i) { acc[0][i] = 0.f; acc[1][i] = 0.f; }
#pragma unroll
    for (int sx = 0; sx < 16; ++sx)
#pragma unroll
      for (int T = 0; T < 2; ++T) acc[T] = __builtin_amdgcn_mfma_f32_32x32x16_bf16(wf[sx][T], a[sx], acc[T], 0, 0, 0);
    STAMP(15);
    LAS f32x4* red = (LAS f32x4*)(L + RED);
#pragma unroll
    for (int j = 0; j < 4; ++j) {
      const f32x16& o = acc[kh ^ 1];
      red[(((mh * 2 + kh) * 4) + j) * 64 + lane] = f32x4{o[4 * j], o[4 * j + 1], o[4 * j + 2], o[4 * j + 3]};
    }
    WAITL0();
    if (lane == 0) __hip_atomic_fetch_add(lc + 2, 1u, __ATOMIC_RELAXED, __HIP_MEMORY_SCOPE_WORKGROUP);
    while (__hip_atomic_load(lc + 2, __ATOMIC_RELAXED, __HIP_MEMORY_SCOPE_WORKGROUP) < 4u) {}
    float s1 = 0.f, s2 = 0.f;
#pragma unroll
    for (int j = 0; j < 4; ++j) {
      const f32x4 o = red[(((mh * 2 + (kh ^ 1)) * 4) + j) * 64 + lane];
      const f32x16& m = acc[kh];
      const unsigned y01 = pack2(m[4 * j] + o[0], m[4 * j + 1] + o[1]), y23 = pack2(m[4 * j + 2] + o[2], m[4 * j + 3] + o[3]);
      hp[2 * j] = pack2(bflo(xr[j][0]) + bflo(y01), bfhi(xr[j][0]) + bfhi(y01));
      hp[2 * j + 1] = pack2(bflo(xr[j][1]) + bflo(y23), bfhi(xr[j][1]) + bfhi(y23));
      s1 = dot2(hp[2 * j], o2, s1); s1 = dot2(hp[2 * j + 1], o2, s1);
      s2 = dot2(hp[2 * j], hp[2 * j], s2); s2 = dot2(hp[2 * j + 1], hp[2 * j + 1], s2);
    }
    {
      const unsigned ho = WS_HF + ((4 * rt + 2 * mh + (tk >> 4)) * 16 + 2 * ct + kh) * 1024 + (hf * 16 + (tk & 15)) * 16;
#pragma unroll
      for (int jj = 0; jj < 2; ++jj) {
        const auto a0 = __builtin_amdgcn_permlane32_swap(hp[4 * jj], hp[4 * jj + 2], false, false);
        const auto a1 = __builtin_amdgcn_permlane32_swap(hp[4 * jj + 1], hp[4 * jj + 3], false, false);
        const v4u o = {a0[0], a1[0], a0[1], a1[1]};
        __builtin_amdgcn_raw_buffer_store_b128(o, rws, ho + jj * 512, 0, AUX_SC1);
      }
      asm volatile("s_nop 3" : "+v"(s1), "+v"(s2));
      float x0, x1;
      swap32(s1, x0, x1); s1 = x0 + x1;
      swap32(s2, x0, x1); s2 = x0 + x1;
      if (lane < 32) {
        v2u sv = {asu(s1), asu(s2)};
        __builtin_amdgcn_raw_buffer_store_b64(sv, rws, WS_ST + (((4 * rt + 2 * mh + (tk >> 4)) * 16 + 2 * ct + kh) * 16 + (tk & 15)) * 8, 0, AUX_SC1);
      }
    }
    STAMP(16);
    WAITV(0);
    if (lane == 0) {
      const unsigned old = __hip_atomic_fetch_add(lc + 1, 1u, __ATOMIC_RELAXED, __HIP_MEMORY_SCOPE_WORKGROUP);
      if (old == 3u) __hip_atomic_fetch_add(fl + (FL_C2 + rt * 128) / 4, 1u, RLX_AGENT);
    }
    STAMP(17);
  }
  WAITV(0);
  WAITL0();
  BAR();
  if (has3) {
    {
      f32x4 cs = z4;
      const unsigned wb = (wtr_lane(lane, w & 3) ^ ((w >> 2) << 6)) + (w >> 2) * 4096;
      bf16x8 t[16];
#pragma unroll
      for (int ks = 0; ks < 16; ++ks) t[ks] = tr_frag(L + ks * 8192 + wb);
      SB();
#pragma unroll
      for (int ks = 0; ks < 16; ++ks) cs = mfma32(t[ks], ones, cs);
      if (r == 0) *(LAS f32x4*)(L + P3_CS + (w * 16 + 4 * g) * 4) = cs;
    }
    STAMP(18);
    for (unsigned spins = 0;;) {
      const bool ok = __hip_atomic_load(fl + (FL_C2 + (4 * pn + (w >> 1)) * 128) / 4, RLX_AGENT) >= 8u;
      if (__all(ok)) break;
      if (poll_fail(spins, fl, lane)) break;
    }
    __builtin_amdgcn_fence(__ATOMIC_ACQUIRE, "wavefront");
    STAMP(19);
    const int mt0 = 16 * pn + 2 * w, tk = lane & 31, hf = lane >> 5;
    bf16x8 af[32];
    v2u sp[8];
    const unsigned hsrc = WS_HF + (mt0 + (tk >> 4)) * 16384 + (hf * 16 + (tk & 15)) * 16;
    auto ldh = [&](int s) { af[s] = __builtin_bit_cast(bf16x8, __builtin_amdgcn_raw_buffer_load_b128(rws, hsrc + (s & 1) * 512, ((((s >> 1) & 8) ^ fl3) | (((s >> 1) + r3) & 7)) * 1024, AUX_SC1)); };
    const unsigned ssrc = WS_ST + (((mt0 + (tk >> 4)) * 16 + 8 * hf) * 16 + (tk & 15)) * 8;
#pragma unroll
    for (int s = 0; s < 8; ++s) ldh(s);
    f32x16 acc[4];
#pragma unroll
    for (int n = 0; n < 4; ++n)
#pragma unroll
      for (int i = 0; i < 16; ++i) acc[n][i] = 0.f;
    unsigned wl[4], wlb[4];
    {
      const int q = (lane & 15) >> 2, pp = lane & 3, bb = (lane >> 4) & 1;
#pragma unroll
      for (int n = 0; n < 4; ++n) {
        const int pos = (2 * n + bb) ^ (((q >> 1) << 1) | (hf << 2) | 0) ^ ((pp >> 1) << 2);
        wl[n] = (pp >> 1) * 4096 + hf * 1024 + q * 128 + pos * 16 + (pp & 1) * 8;
        wlb[n] = wl[n] + GU_B;
        asm volatile("" : "+v"(wl[n]), "+v"(wlb[n]));
      }
    }
    auto rd = [&](unsigned base, int off) { return __builtin_amdgcn_ds_read_tr16_b64_v4i16((LAS bf16x4*)((LAS unsigned char*)(size_t)base + off)); };
    bf16x4 wh[4][2];
    auto rdM = [&](int M, int h) {
      const int n = M < 64 ? (M & 3) : (M - 64) >> 4, s = M < 64 ? (M >> 2) : ((M - 64) & 15);
      wh[M & 3][h] = rd(M < 64 ? wl[n] : wlb[n], (s >> 1) * 8192 + (s & 1) * 2048 + h * 512);
    };
    auto mmM = [&](int M) {
      const int n = M < 64 ? (M & 3) : (M - 64) >> 4, s = M < 64 ? (M >> 2) : 16 + ((M - 64) & 15);
      acc[n] = __builtin_amdgcn_mfma_f32_32x32x16_bf16(__builtin_shufflevector(wh[M & 3][0], wh[M & 3][1], 0, 1, 2, 3, 4, 5, 6, 7), af[s], acc[n], 0, 0, 0);
    };
#pragma unroll
    for (int M = 0; M < 3; ++M) { rdM(M, 0); rdM(M, 1); }
    SB();
    static_for<0, 64>([&](auto MM) {
      constexpr int M = decltype(MM)::value;
      mmM(M);
      rdM(M + 3, 0); rdM(M + 3, 1);
      if ((M & 1) == 0 && 8 + (M >> 1) < 32) ldh(8 + (M >> 1));
      if (M >= 48 && (M & 1) == 0) sp[(M - 48) >> 1] = __builtin_amdgcn_raw_buffer_load_b64(rws, ssrc + (((M - 48) >> 1) & 7) * 128, 0, AUX_SC1);
      SB();
    });
    STAMP(20);
    float nmean, c1, c3, irs2;
    {
      float s1 = 0.f, s2 = 0.f;
#pragma unroll
      for (int i = 0; i < 8; ++i) { s1 += asf(sp[i][0]); s2 += asf(sp[i][1]); }
      float a, b;
      swap32(s1, a, b); s1 = a + b;
      swap32(s2, a, b); s2 = a + b;
      const float mn = s1 * (1.f / D_);
      irs2 = fmaxf(s2 * (1.f / D_) - mn * mn, 0.f) + LN_EPS;
      nmean = -mn;
      const float rs = __builtin_amdgcn_rsqf(irs2);
      c1 = -2.302208198f * rs; c3 = -0.1029432397f * rs * rs * rs;
      asm volatile("" : "+v"(nmean), "+v"(c1), "+v"(c3), "+v"(irs2));
    }
    WAITL0();
    BAR();
#pragma unroll
    for (int c = 0; c < 5; ++c) issue_wd(c);
    STAMP(21);
    SB();
    const unsigned actoff = WS_ACT + ((mt0 + (tk >> 4)) * 56 + 2 * fs) * 1024 + (hf * 16 + (tk & 15)) * 16;
    f32x4 cv[4];
    float eG[8], eT[8], eZ[8];
    unsigned ob[4];
    auto epi_step = [&](int n, int k) {
      const int st = k >> 3, v = k & 7, gi = (v >> 2) * 8 + (v & 3), ui = gi + 4;
      if (k == 0) {
        cv[0] = *(LAS f32x4*)(L + P3_CS + (n * 16 + 4 * hf) * 4); cv[1] = *(LAS f32x4*)(L + P3_CS + ((4 + n) * 16 + 4 * hf) * 4);
        cv[2] = *(LAS f32x4*)(L + P3_CS + (n * 16 + 8 + 4 * hf) * 4); cv[3] = *(LAS f32x4*)(L + P3_CS + ((4 + n) * 16 + 8 + 4 * hf) * 4);
      }
      else if (st == 1) eG[v] = fmaf(nmean, cv[(v >> 2) * 2][v & 3], acc[n][gi]);
      else if (st == 2) eT[v] = eG[v] * eG[v];
      else if (st == 3) eT[v] = fmaf(eT[v], c3, c1);
      else if (st == 4) eT[v] = eT[v] * eG[v];
      else if (st == 5) eT[v] = __builtin_amdgcn_exp2f(eT[v]);
      else if (st == 6) eZ[v] = fmaf(nmean, cv[(v >> 2) * 2 + 1][v & 3], acc[n][ui]);
      else if (st == 7) eT[v] = fmaf(eT[v], irs2, irs2);
      else if (st == 8) eZ[v] = eZ[v] * eG[v];
      else if (st == 9) eT[v] = __builtin_amdgcn_rcpf(eT[v]);
      else if (st == 10) eZ[v] = eZ[v] * eT[v];
      else if (k >= 88 && k < 92) ob[k - 88] = pack2(eZ[2 * (k - 88)], eZ[2 * (k - 88) + 1]);
      else if (k == 92) {
        const auto a0 = __builtin_amdgcn_permlane32_swap(ob[0], ob[2], false, false);
        const auto a1 = __builtin_amdgcn_permlane32_swap(ob[1], ob[3], false, false);
        const v4u ov = {a0[0], a1[0], a0[1], a1[1]};
        __builtin_amdgcn_raw_buffer_store_b128(ov, rws, actoff + (n >> 1) * 1024 + (n & 1) * 512, 0, AUX_SC1);
      }
    };
    static_for<64, 128>([&](auto MM) {
      constexpr int M = decltype(MM)::value;
      constexpr int n = (M - 64) >> 4, sl = (M - 64) & 15;
      mmM(M);
      if (M + 3 < 128) { rdM(M + 3, 0); rdM(M + 3, 1); }
      if (n > 0) {
        if (sl == 0) epi_step(n - 1, 0);
        else if (sl <= 14) {
#pragma unroll
          for (int j = 0; j < 6; ++j) epi_step(n - 1, 8 + 6 * (sl - 1) + j);
        } else epi_step(n - 1, 92);
      }
      SB();
    });
    STAMP(22);
    epi_step(3, 0);
    static_for<8, 93>([&](auto KK) { epi_step(3, decltype(KK)::value); });
    STAMP(29);
    WAITV(0);
    BAR();
    if (tid == 0) __hip_atomic_fetch_add(fl + (FL_C3 + pn * 128) / 4, 1u, RLX_AGENT);
    STAMP(30);
  } else {
    issue_wd(0);
    WAITV(0);
    BAR();
  }
#define LC_ADD(i) do { WAITL0(); if (lane == 0) __hip_atomic_fetch_add(lc + (i), 1u, __ATOMIC_RELAXED, __HIP_MEMORY_SCOPE_WORKGROUP); } while (0)
#define LC_WAIT(i) do { while (__hip_atomic_load(lc + (i), __ATOMIC_RELAXED, __HIP_MEMORY_SCOPE_WORKGROUP) < 4u) __builtin_amdgcn_s_sleep(1); asm volatile("" ::: "memory"); } while (0)
  if (w >= 4) {
#pragma unroll
    for (int c = 5; c < 9; ++c) issue_wd4(c, w - 4);
    LC_WAIT(3);
#pragma unroll
    for (int c = 9; c < 13; ++c) issue_wd4(c, w - 4);
    WAITV(16);
    LC_ADD(4);
    WAITV(0);
    LC_ADD(9);
    LC_WAIT(5);
    issue_wd4(13, w - 4);
    WAITV(0);
    LC_ADD(6);
  } else {
    const int ct = c8;
    unsigned wl[2], wh[2], wx[2];
#pragma unroll
    for (int T = 0; T < 2; ++T) {
      wl[T] = wt[T] + kh * 8192; wh[T] = wl[T] + 65536; wx[T] = wl[T] + 131072;
      asm volatile("" : "+v"(wl[T]), "+v"(wh[T]), "+v"(wx[T]));
    }
    auto wdfrag = [&](int sx, int T) {
      const int sl = wd_slot(sx >> 2), o = ((sx >> 1) & 1) * 4096 + (sx & 1) * 2048;
      return sl >= 131072 ? tr_frag((LAS unsigned char*)(size_t)wx[T] + o)
           : sl >= 65536 ? tr_frag((LAS unsigned char*)(size_t)wh[T] + (sl - 65536) + o) : tr_frag((LAS unsigned char*)(size_t)wl[T] + sl + o);
    };
    bf16x8 wr[16][2];
#pragma unroll
    for (int sx = 0; sx < 16; ++sx)
#pragma unroll
      for (int T = 0; T < 2; ++T) wr[sx][T] = wdfrag(sx, T);
    LC_ADD(3);
    for (unsigned spins = 0;;) {
      const bool ok = __hip_atomic_load(fl + (FL_C3 + bx * 128) / 4, RLX_AGENT) >= 28u;
      if (__all(ok)) break;
      if (poll_fail(spins, fl, lane)) break;
    }
    __builtin_amdgcn_fence(__ATOMIC_ACQUIRE, "wavefront");
    if (w == 0 && bi == 31 && lane == (bx & 1)) __hip_atomic_store(fl + (FL_E + bx * 256) / 4, tagv, RLX_AGENT);
    STAMP(31);
    constexpr int AW = 10;
    bf16x8 a[AW];
    const unsigned asrc = WS_ACT + ((4 * rt + 2 * mh + (tk >> 4)) * 56 + kh * 28) * 1024 + (hf * 16 + (tk & 15)) * 16;
    auto lda = [&](int sx) { const int c = sx >> 2, pc_ = c + r4 >= 14 ? c + r4 - 14 : c + r4;
      a[sx % AW] = __builtin_bit_cast(bf16x8, __builtin_amdgcn_raw_buffer_load_b128(rws, asrc + ((sx >> 1) & 1) * 1024 + (sx & 1) * 512, pc_ * 2048, AUX_SC1)); };
#pragma unroll
    for (int sx = 0; sx < AW; ++sx) lda(sx);
    f32x16 acc[2];
#pragma unroll
    for (int i = 0; i < 16; ++i) { acc[0][i] = 0.f; acc[1][i] = 0.f; }
    bf16x8 wf[2][2];
    SB();
    static_for<0, 56>([&](auto SS) {
      constexpr int sx = decltype(SS)::value;
      if (sx == 19) LC_WAIT(4);
      if (sx == 35) LC_WAIT(9);
      if (sx == 20) LC_ADD(5);
      if (sx == 51) LC_WAIT(6);
      if (sx + 1 >= 16 && sx + 1 < 56) { wf[(sx + 1) & 1][0] = wdfrag(sx + 1, 0); wf[(sx + 1) & 1][1] = wdfrag(sx + 1, 1); }
      acc[0] = __builtin_amdgcn_mfma_f32_32x32x16_bf16(sx < 16 ? wr[sx & 15][0] : wf[sx & 1][0], a[sx % AW], acc[0], 0, 0, 0);
      acc[1] = __builtin_amdgcn_mfma_f32_32x32x16_bf16(sx < 16 ? wr[sx & 15][1] : wf[sx & 1][1], a[sx % AW], acc[1], 0, 0, 0);
      if (sx + AW < 56) lda(sx + AW);
      SB();
    });
    STAMP(33);
    LAS f32x4* red = (LAS f32x4*)(L + SM - 16384);
    LC_ADD(7);
    LC_WAIT(7);
#pragma unroll
    for (int j = 0; j < 4; ++j) {
      const f32x16& o = acc[kh ^ 1];
      red[(((mh * 2 + kh) * 4) + j) * 64 + lane] = f32x4{o[4 * j], o[4 * j + 1], o[4 * j + 2], o[4 * j + 3]};
    }
    LC_ADD(8);
    LC_WAIT(8);
    unsigned op[8];
#pragma unroll
    for (int j = 0; j < 4; ++j) {
      const f32x4 o = red[(((mh * 2 + (kh ^ 1)) * 4) + j) * 64 + lane];
      const f32x16& m = acc[kh];
      const unsigned y01 = pack2(m[4 * j] + o[0], m[4 * j + 1] + o[1]), y23 = pack2(m[4 * j + 2] + o[2], m[4 * j + 3] + o[3]);
      op[2 * j] = pack2(bflo(hp[2 * j]) + bflo(y01), bfhi(hp[2 * j]) + bfhi(y01));
      op[2 * j + 1] = pack2(bflo(hp[2 * j + 1]) + bflo(y23), bfhi(hp[2 * j + 1]) + bfhi(y23));
    }
    unsigned short* orow = p.out + (size_t)(64 * rt + 32 * mh + tk) * D_ + 64 * ct + 32 * kh + 8 * hf;
#pragma unroll
    for (int jj = 0; jj < 2; ++jj) {
      const auto a0 = __builtin_amdgcn_permlane32_swap(op[4 * jj], op[4 * jj + 2], false, false);
      const auto a1 = __builtin_amdgcn_permlane32_swap(op[4 * jj + 1], op[4 * jj + 3], false, false);
      const v4u o = {a0[0], a1[0], a0[1], a1[1]};
      *(GAS v4u*)(orow + 16 * jj) = o;
    }
    STAMP(34);
  }
  STAMP_END();
}

#if defined(__HIP_DEVICE_COMPILE__)
#pragma clang attribute pop
#endif
extern "C" void kernel_launch(void* const* d_in, const int* in_sizes, int n_in, void* d_out, int out_size, void* d_ws, size_t ws_size, hipStream_t stream) {
  Params p;
  p.x = (const unsigned short*)d_in[0];
  p.wqkv = (const unsigned short*)d_in[1];
  p.wo = (const unsigned short*)d_in[2];
  p.wg = (const unsigned short*)d_in[4];
  p.wu = (const unsigned short*)d_in[5];
  p.wd = (const unsigned short*)d_in[6];
  p.out = (unsigned short*)d_out;
  p.ws = (unsigned char*)d_ws;
  constexpr int LDS = 160 * 1024;
  (void)hipFuncSetAttribute((const void*)k_fused, hipFuncAttributeMaxDynamicSharedMemorySize, LDS);
  k_fused<<<256, 512, LDS, stream>>>(p);
}
```

```cpp
#include <hip/hip_runtime.h>
#include <hip/hip_bf16.h>

#define GAS __attribute__((address_space(1)))
#define LAS __attribute__((address_space(3)))
typedef short bf16x8 __attribute__((ext_vector_type(8)));
typedef short bf16x4 __attribute__((ext_vector_type(4)));
typedef __bf16 bf2 __attribute__((ext_vector_type(2)));
typedef float f32x4 __attribute__((ext_vector_type(4)));
typedef float f32x2 __attribute__((ext_vector_type(2)));
typedef float f32x16 __attribute__((ext_vector_type(16)));
typedef unsigned v4u __attribute__((ext_vector_type(4)));
typedef unsigned v2u __attribute__((ext_vector_type(2)));
#define DI __device__ __forceinline__

constexpr int S_ = 2048, D_ = 512, F_ = 1792;
constexpr int AUX_SC1 = 16;
constexpr unsigned RSRC3 = 0x00020000u;
constexpr unsigned WS_ATT = 0u;
constexpr unsigned WS_HF = 2u << 20;
constexpr unsigned WS_ACT = 4u << 20;
constexpr unsigned WS_ST = 12u << 20;
constexpr unsigned WS_FL = 13u << 20;
constexpr unsigned FL_E = 0, FL_F1 = 4096, FL_C2 = 16384, FL_C3 = 24576, FL_TMO = 36864;
constexpr unsigned WS_BYTES = 14u << 20;
typedef GAS unsigned gu32;
#define RLX_AGENT __ATOMIC_RELAXED, __HIP_MEMORY_SCOPE_AGENT
constexpr unsigned SPIN_CAP = 1u << 18;

struct Params {
  const unsigned short* x;
  const unsigned short* wqkv;
  const unsigned short* wo;
  const unsigned short* wg;
  const unsigned short* wu;
  const unsigned short* wd;
  unsigned short* out;
  unsigned char* ws;
};

#if defined(__HIP_DEVICE_COMPILE__)
#pragma clang attribute push (__attribute__((target("no-packed-fp32-ops"))), apply_to = function)
#endif
template <int V> struct IC { static constexpr int value = V; };
template <int A, int B, class F> __device__ __forceinline__ void static_for(F&& f) { if constexpr (A < B) { f(IC<A>{}); static_for<A + 1, B>(f); } }
DI float asf(unsigned u) { return __builtin_bit_cast(float, u); }
DI unsigned asu(float f) { return __builtin_bit_cast(unsigned, f); }
DI f32x4 mfma32(bf16x8 a, bf16x8 b, f32x4 c) { return __builtin_amdgcn_mfma_f32_16x16x32_bf16(a, b, c, 0, 0, 0); }
DI f32x4 mfma16(bf16x4 a, bf16x4 b, f32x4 c) { return __builtin_amdgcn_mfma_f32_16x16x16bf16_1k(a, b, c, 0, 0, 0); }
DI unsigned pack2(float lo, float hi) { bf2 p; p[0] = (__bf16)lo; p[1] = (__bf16)hi; return __builtin_bit_cast(unsigned, p); }
DI float bflo(unsigned u) { return __builtin_bit_cast(float, u << 16); }
DI float bfhi(unsigned u) { return __builtin_bit_cast(float, u & 0xffff0000u); }
DI unsigned ones2() { unsigned o = 0x3F803F80u; asm volatile("" : "+v"(o)); return o; }
DI float dot2(unsigned a, unsigned b, float c) { return __builtin_amdgcn_fdot2_f32_bf16(__builtin_bit_cast(bf2, a), __builtin_bit_cast(bf2, b), c, false); }
DI __amdgpu_buffer_rsrc_t mkrs(const void* p, unsigned bytes) { return __builtin_amdgcn_make_buffer_rsrc((void*)p, 0, bytes, RSRC3); }
DI void dma16(__amdgpu_buffer_rsrc_t rs, unsigned dst, unsigned voff, unsigned soff) {
  asm volatile("s_mov_b32 m0, %0\n\ts_nop 0\n\tbuffer_load_dwordx4 %1, %2, %3 offen lds"
               :: "s"(dst), "v"(voff), "s"(rs), "s"(soff) : "m0", "memory");
}
template <int SS> DI void dma16x4(__amdgpu_buffer_rsrc_t rs, unsigned dst, unsigned v0, unsigned v1, unsigned soff) {
  unsigned t;
  asm volatile("s_mov_b32 m0, %1\n\ts_add_u32 %0, %5, %6\n\t"
               "buffer_load_dwordx4 %2, %4, %5 offen lds\n\t"
               "buffer_load_dwordx4 %3, %4, %0 offen offset:1024 lds\n\ts_add_u32 %0, %0, %6\n\t"
               "buffer_load_dwordx4 %2, %4, %0 offen offset:2048 lds\n\ts_add_u32 %0, %0, %6\n\t"
               "buffer_load_dwordx4 %3, %4, %0 offen offset:3072 lds"
               : "=&s"(t) : "s"(dst), "v"(v0), "v"(v1), "s"(rs), "s"(soff), "n"(SS - 1024) : "m0", "scc", "memory");
}
template <int LS> DI void dma16x3(__amdgpu_buffer_rsrc_t rs, unsigned dst, unsigned v, unsigned soff) {
  asm volatile("s_mov_b32 m0, %0\n\ts_nop 0\n\t"
               "buffer_load_dwordx4 %1, %2, %3 offen lds\n\ts_add_u32 m0, m0, %4\n\ts_nop 0\n\t"
               "buffer_load_dwordx4 %1, %2, %3 offen offset:1024 lds\n\ts_add_u32 m0, m0, %4\n\ts_nop 0\n\t"
               "buffer_load_dwordx4 %1, %2, %3 offen offset:2048 lds"
               :: "s"(dst), "v"(v), "s"(rs), "s"(soff), "n"(LS - 1024) : "m0", "scc", "memory");
}
DI bf16x8 lds_frag(LAS unsigned char* p) { return *(LAS bf16x8*)p; }
DI bf16x8 tr_frag(LAS unsigned char* p) {
  bf16x4 lo = __builtin_amdgcn_ds_read_tr16_b64_v4i16((LAS bf16x4*)p);
  bf16x4 hi = __builtin_amdgcn_ds_read_tr16_b64_v4i16((LAS bf16x4*)(p + 512));
  return __builtin_shufflevector(lo, hi, 0, 1, 2, 3, 4, 5, 6, 7);
}
DI void swap16(float v, float& a, float& b) {
  const unsigned u = asu(v);
  auto r = __builtin_amdgcn_permlane16_swap(u, u, false, false);
  a = asf(r[0]); b = asf(r[1]);
}
DI void swap32(float v, float& a, float& b) {
  const unsigned u = asu(v);
  auto r = __builtin_amdgcn_permlane32_swap(u, u, false, false);
  a = asf(r[0]); b = asf(r[1]);
}
DI float xsum_g(float v) {
  const f32x4 z = {0.f, 0.f, 0.f, 0.f};
  asm volatile("s_nop 3" : "+v"(v));
  return __builtin_amdgcn_mfma_f32_16x16x4f32(1.0f, v, z, 0, 0, 0)[0];
}
DI float xmax_g(float v) {
  float a, b;
  swap16(v, a, b); v = fmaxf(a, b);
  swap32(v, a, b); return fmaxf(a, b);
}
#define WAITV(n) asm volatile("s_waitcnt vmcnt(" #n ")" ::: "memory")
#define WAITL0() asm volatile("s_waitcnt lgkmcnt(0)" ::: "memory")
#define BAR() asm volatile("s_barrier" ::: "memory")
#define SB() __builtin_amdgcn_sched_barrier(0)
#define SG(mask, n) __builtin_amdgcn_sched_group_barrier(mask, n, 0)
#define M_MFMA 0x8
#define M_DSR 0x100
#define M_DSW 0x200
#define M_VALU 0x2
#define M_TRANS 0x400
#define M_VMEMR 0x20
constexpr float LN_EPS = 1e-5f;
#define STAMP_DECL do {} while (0)
#define STAMP(i) do {} while (0)
#define STAMP_END() do {} while (0)

DI unsigned wtr_lane(int lane, int nt) {
  const int g = lane >> 4, q = (lane & 15) >> 2, p = lane & 3;
  const int sg = (q >> 1) | ((g & 1) << 1);
  return g * 1024 + q * 128 + ((nt ^ sg) << 5) + p * 8;
}
DI unsigned wdma32_lane(int lane, unsigned pitch) {
  const int r8 = lane >> 3, pos = lane & 7;
  return r8 * pitch + ((pos ^ (((r8 >> 1) & 1) << 2)) << 4);
}
DI unsigned wdma_lane(int lane, unsigned pitch, int odd) {
  const int r8 = lane >> 3, pos = lane & 7;
  return r8 * pitch + ((pos ^ (((r8 >> 1) & 1) << 1) ^ (odd << 2)) << 4);
}

#define PSLEEP 1
#define ZW1 0
#define ZW2 0
#define ZW3 0
#define ZW4 0
#define ZX 0
#define ZA 0
constexpr int P1_X = 0;
constexpr int P1_W = 81920;
constexpr int SM = 155648;
constexpr int P1_PART = SM;
constexpr int P1_CS = SM + 2560;
constexpr int P1_FIN = SM + 4096;
constexpr int LC = SM + 7168;
constexpr int P3_CS = SM;
constexpr int WO_IMG = 65536;
constexpr int RED = 131072;
constexpr int GU_A = 0;
constexpr int GU_B = 65536;
DI constexpr int wd_slot(int c) { return (c % 9) == 0 ? 131072 : ((c % 9) - 1) * 16384; }

DI bool poll_fail(unsigned& spins, gu32* fl, int lane) {
  __builtin_amdgcn_s_sleep(PSLEEP);
  if (++spins > SPIN_CAP) { if (lane == 0) __hip_atomic_store(fl + FL_TMO / 4, 1u, RLX_AGENT); return true; }
  return false;
}

__global__ __launch_bounds__(512, 2) void k_fused(Params p) {
  extern __shared__ __attribute__((aligned(16))) unsigned char smem[];
  LAS unsigned char* L = (LAS unsigned char*)smem;
  const int tid = threadIdx.x, lane = tid & 63, w = __builtin_amdgcn_readfirstlane(tid >> 6);
  const int bx = blockIdx.x & 7, bi = blockIdx.x >> 3, rt = 4 * bx + (bi >> 3), c8 = bi & 7;
  const int pr = bx >> 1, pc = bx & 1;
  const int r = lane & 15, g = lane >> 4;
  const __amdgpu_buffer_rsrc_t rws = mkrs(p.ws, WS_BYTES);
  const __amdgpu_buffer_rsrc_t rwl = mkrs(p.ws, ZA ? 0 : WS_BYTES);
  gu32* fl = (gu32*)(p.ws + WS_FL);
  const f32x4 z4 = {0.f, 0.f, 0.f, 0.f};
  const bf16x8 ones = {0x3F80, 0x3F80, 0x3F80, 0x3F80, 0x3F80, 0x3F80, 0x3F80, 0x3F80};
  const unsigned o2 = ones2();
  const int rt1 = 8 * pr + (bi >> 2), hg = 4 * pc + (bi & 3);
  const bool has3 = bi < 28;
  const int pn = 2 * pr + (bi >= 14 ? 1 : 0), fs = 14 * pc + (bi >= 14 ? bi - 14 : bi);
  const int r2 = (c8 + 2 * (rt & 3)) & 7, r3 = fs & 7, fl3 = bi >= 14 ? 8 : 0, r4 = (2 * c8 + 3 * (rt & 3)) % 14;
  if (tid < 16) ((LAS unsigned*)(L + LC))[tid] = 0u;
  STAMP_DECL;
  STAMP(0);
  const unsigned tagv = __hip_atomic_load(fl + (FL_E + (2 * pr + (lane & 1)) * 256) / 4, RLX_AGENT) + 1u;
  if (w == 0) {
    if (hg == 0 && lane == 0) {
      __hip_atomic_store(fl + (FL_C2 + rt1 * 128) / 4, 0u, RLX_AGENT);
      if ((rt1 & 3) == 0) { for (int i = 0; i < 8; ++i) __hip_atomic_store(fl + (FL_C3 + (rt1 >> 2) * 1024 + i * 128) / 4, 0u, RLX_AGENT); }
    }
  }
#define LC_ADD(i) do { WAITL0(); if (lane == 0) __hip_atomic_fetch_add(lc + (i), 1u, __ATOMIC_RELAXED, __HIP_MEMORY_SCOPE_WORKGROUP); } while (0)
#define LC_WAITN(i, n) do { while (__hip_atomic_load(lc + (i), __ATOMIC_RELAXED, __HIP_MEMORY_SCOPE_WORKGROUP) < (n)) __builtin_amdgcn_s_sleep(1); asm volatile("" ::: "memory"); } while (0)
#define LC_WAIT(i) LC_WAITN(i, 4u)
  LAS unsigned* lc = (LAS unsigned*)(L + LC);
  const int tk = lane & 31, hf = lane >> 5, mh = w & 1, kh = (w >> 1) & 1;
  unsigned wt[2];
  {
    const int q = (lane & 15) >> 2, pp = lane & 3, bb = (lane >> 4) & 1;
#pragma unroll
    for (int T = 0; T < 2; ++T) wt[T] = hf * 1024 + q * 128 + (((4 * T + 2 * bb + (pp >> 1)) ^ ((q >> 1) << 2)) << 4) + (pp & 1) * 8;
  }
  bf16x8 wf2[16][2];
  v2u xr[4];
  {
    const int hh = w & 3, rh = w >> 2, rot = (bi + (bi >> 2) + 2 * pr) & 7;
    const __amdgpu_buffer_rsrc_t rx = mkrs(p.x, ZX ? 0 : S_ * D_ * 2), rw = mkrs(p.wqkv, ZW1 ? 0 : D_ * 3 * D_ * 2);
    const int r8 = lane >> 3, pos = lane & 7;
    const unsigned xv0 = r8 * 1024 + ((pos ^ (r8 >> 1)) << 4), xv1 = xv0 ^ 64;
    auto issue_x = [&](int half) {
      const int c = 4 * half + (w >> 1);
#pragma unroll
      for (int i = 0; i < 5; ++i) {
        const int P = 5 * (w & 1) + i;
        int row = 64 * rt1 - 16 + 8 * P;
        row = row < 0 ? 0 : row;
        dma16(rx, P1_X + (c * 10 + P) * 1024, (P & 1) ? xv1 : xv0, row * 1024 + ((c + rot) & 7) * 128);
      }
    };
    const unsigned wv = wdma_lane(lane, 3072, w & 1);
    auto issue_w = [&](int c) {
      dma16x3<8192>(rw, P1_W + (c % 3) * 24576 + w * 1024, wv, (((c + rot) & 7) * 64 + 8 * w) * 3072 + hg * 128);
    };
    issue_x(0); issue_w(0); issue_w(1);
    STAMP(1);
    f32x4 aq[2] = {z4, z4}, ak[3] = {z4, z4, z4}, av[3] = {z4, z4, z4}, aqh = z4;
    float s1 = 0.f, s2 = 0.f;
    const unsigned xlane = (r >> 3) * 1024 + (r & 7) * 128 + ((g ^ ((r >> 1) & 7)) << 4);
    unsigned xl0 = xlane + 2 * rh * 2048, xl1 = xl0 ^ 64;
    const int stile = rh == 0 ? (hh < 3 ? hh : 0) : (hh < 2 ? 3 + hh : 3);
    unsigned xs0 = xlane + stile * 2048, xs1 = xs0 ^ 64;
    unsigned wlA = wtr_lane(lane, hh) + P1_W, wlB = wlA + 49152;
    unsigned xh0 = xl0 + 40960, xh1 = xl1 + 40960, xsh0 = xs0 + 40960, xsh1 = xs1 + 40960;
    asm volatile("" : "+v"(xl0), "+v"(xl1), "+v"(xs0), "+v"(xs1), "+v"(wlA), "+v"(wlB), "+v"(xh0), "+v"(xh1), "+v"(xsh0), "+v"(xsh1));
    bf16x8 fx[2][4];
    bf16x4 fw[2][3][2];
    auto rd1 = [&](int K, int j) {
      const int c = K >> 1, sx = K & 1, bf = K & 1;
      if (j < 6) {
        const int part = j >> 1, h = j & 1;
        const unsigned base = (c % 3) == 2 ? wlB : wlA;
        const int off = ((c % 3) == 2 ? 0 : (c % 3) * 24576) + part * 8192 + sx * 4096 + h * 512;
        fw[bf][part][h] = __builtin_amdgcn_ds_read_tr16_b64_v4i16((LAS bf16x4*)((LAS unsigned char*)(size_t)base + off));
      } else if (j < 9) {
        const int t = j == 6 ? 1 : j == 7 ? 2 : 0;
        const unsigned base = c < 4 ? (sx ? xl1 : xl0) : (sx ? xh1 : xh0);
        fx[bf][t] = lds_frag((LAS unsigned char*)(size_t)base + (c & 3) * 10240 + t * 2048);
      } else {
        const unsigned base = c < 4 ? (sx ? xs1 : xs0) : (sx ? xsh1 : xsh0);
        fx[bf][3] = lds_frag((LAS unsigned char*)(size_t)base + (c & 3) * 10240);
      }
    };
    constexpr int RORD[10] = {0, 1, 6, 7, 2, 3, 8, 4, 5, 9};
    auto wfrag = [&](int bf, int part) { return __builtin_shufflevector(fw[bf][part][0], fw[bf][part][1], 0, 1, 2, 3, 4, 5, 6, 7); };
    auto mm1 = [&](int K, int j) {
      const int bf = K & 1;
      if (j == 0) aq[0] = mfma32(wfrag(bf, 0), fx[bf][1], aq[0]);
      else if (j == 1) aq[1] = mfma32(wfrag(bf, 0), fx[bf][2], aq[1]);
      else if (j == 2) ak[1] = mfma32(wfrag(bf, 1), fx[bf][1], ak[1]);
      else if (j == 3) ak[2] = mfma32(wfrag(bf, 1), fx[bf][2], ak[2]);
      else if (j == 4) ak[0] = mfma32(wfrag(bf, 1), fx[bf][0], ak[0]);
      else if (j == 5) av[0] = mfma32(fx[bf][0], wfrag(bf, 2), av[0]);
      else if (j == 6) av[1] = mfma32(fx[bf][1], wfrag(bf, 2), av[1]);
      else av[2] = mfma32(fx[bf][2], wfrag(bf, 2), av[2]);
    };
    auto ones_row = [&](int half) {
      if ((w & 1) == 0 && lane < 8) *(LAS v4u*)(L + P1_X + (4 * half + (w >> 1)) * 10240 + lane * 16) = v4u{0x3F803F80u, 0x3F803F80u, 0x3F803F80u, 0x3F803F80u};
    };
    f32x4 b0, b1;
#pragma unroll
    for (int i = 0; i < 4; ++i) {
      const int d0 = 16 + r - (4 * g + i), d1 = r - (4 * g + i);
      b0[i] = (d0 <= 14 && !(d0 & 1)) ? 0.f : -1.0e30f;
      b1[i] = (d1 >= 0 && d1 <= 14 && !(d1 & 1)) ? 0.f : -1.0e30f;
    }
    asm volatile("" : "+v"(b0), "+v"(b1));
    WAITV(3);
    ones_row(0);
    WAITL0();
    BAR();
    STAMP(2);
    issue_w(2);
    v4u wu_[3];
    {
      const unsigned b = blockIdx.x;
      const GAS v4u* q0 = w < 7 ? (const GAS v4u*)p.wg + b * 448 + w * 64 + lane : (const GAS v4u*)p.wo + b * 128 + lane;
      const GAS v4u* q1 = w < 7 ? (const GAS v4u*)p.wu + b * 448 + w * 64 + lane : (const GAS v4u*)p.wo + b * 128 + 64 + lane;
      const GAS v4u* q2 = (const GAS v4u*)p.wd + b * 448 + (w < 7 ? w : 6) * 64 + lane;
      asm volatile("global_load_dwordx4 %0, %3, off\n\tglobal_load_dwordx4 %1, %4, off\n\tglobal_load_dwordx4 %2, %5, off"
                   : "=&v"(wu_[0]), "=&v"(wu_[1]), "=&v"(wu_[2]) : "v"(q0), "v"(q1), "v"(q2) : "memory");
    }
#pragma unroll
    for (int j = 0; j < 10; ++j) rd1(0, RORD[j]);
    SB();
    static_for<0, 16>([&](auto KK) {
      constexpr int K = decltype(KK)::value, c = K >> 1;
      if constexpr ((K & 1) == 1 && c < 7) {
        if (c == 0) WAITV(6);
        else if (c == 1) WAITV(11);
        else if (c == 2) WAITV(8);
        else if (c == 6) WAITV(0);
        else WAITV(3);
        if (c == 2) asm volatile("" :: "v"(wu_[0]), "v"(wu_[1]), "v"(wu_[2]));
        if (c == 3) ones_row(1);
        WAITL0();
        BAR();
        STAMP(3 + c);
        if (c + 3 < 8) issue_w(c + 3);
        if (c == 0) issue_x(1);
        SB();
      }
      static_for<0, 8>([&](auto JJ) {
        constexpr int j = decltype(JJ)::value;
        mm1(K, j);
        if (K + 1 < 16) { rd1(K + 1, RORD[j]); if (j >= 6) rd1(K + 1, RORD[j + 2]); }
        { const v4u u = __builtin_bit_cast(v4u, fx[K & 1][3]);
          if (j & 1) s2 = dot2(u[j >> 1], u[j >> 1], s2); else s1 = dot2(u[j >> 1], o2, s1); }
        SB();
      });
      if (rh == 0) aqh = mfma32(wfrag(K & 1, 0), fx[K & 1][0], aqh);
      SB();
    });
    STAMP(10);
    LAS f32x2* fin = (LAS f32x2*)(L + P1_FIN);
    LAS float* csx = (LAS float*)(L + P1_CS);
    {
      const float a = xsum_g(s1), b = xsum_g(s2);
      const float mean = a * (1.f / D_);
      const float rstd = __builtin_amdgcn_rsqf(fmaxf(b * (1.f / D_) - mean * mean, 0.f) + LN_EPS);
      if (lane < 16 && (rh == 0 ? hh < 3 : hh < 2)) fin[stile * 16 + lane] = f32x2{mean, rstd};
      if (rh == 0) {
        if (r == 0) { *(LAS f32x4*)&csx[hh * 48 + 4 * g] = aqh; *(LAS f32x4*)&csx[hh * 48 + 16 + 4 * g] = ak[0]; }
        if (lane < 16) csx[hh * 48 + 32 + lane] = av[0][0];
      }
    }
    WAITL0();
    BAR();
    {
      const __amdgpu_buffer_rsrc_t rwo = mkrs(p.wo, ZW2 ? 0 : D_ * D_ * 2);
      const unsigned a0 = wdma32_lane(lane, 1024), a1 = a0;
      dma16x4<8192>(rwo, WO_IMG + w * 8192, a0, a1, (((2 * w) & 8) | ((2 * w + r2) & 7)) * 32768 + c8 * 128);
      dma16x4<8192>(rwo, WO_IMG + w * 8192 + 4096, a0, a1, (((2 * w) & 8) | ((2 * w + 1 + r2) & 7)) * 32768 + c8 * 128);
    }
    const f32x4 cq = *(LAS f32x4*)&csx[hh * 48 + 4 * g], ck = *(LAS f32x4*)&csx[hh * 48 + 16 + 4 * g];
    const float cv0 = csx[hh * 48 + 32 + r];
    STAMP(11);
    bf16x4 Qp[2], Kp[3], Vp[3];
#pragma unroll
    for (int t = 0; t < 3; ++t) {
      const f32x2 st = fin[(2 * rh + t) * 16 + r];
      const f32x4 s01 = *(LAS f32x4*)&fin[(2 * rh + t) * 16 + 4 * g], s23 = *(LAS f32x4*)&fin[(2 * rh + t) * 16 + 4 * g + 2];
      const float nm = -st[0], rs = st[1];
      v2u u;
      u[0] = pack2(fmaf(nm, ck[0], ak[t][0]) * rs, fmaf(nm, ck[1], ak[t][1]) * rs);
      u[1] = pack2(fmaf(nm, ck[2], ak[t][2]) * rs, fmaf(nm, ck[3], ak[t][3]) * rs);
      Kp[t] = __builtin_bit_cast(bf16x4, u);
      if (t >= 1) {
        u[0] = pack2(fmaf(nm, cq[0], aq[t - 1][0]) * rs, fmaf(nm, cq[1], aq[t - 1][1]) * rs);
        u[1] = pack2(fmaf(nm, cq[2], aq[t - 1][2]) * rs, fmaf(nm, cq[3], aq[t - 1][3]) * rs);
        Qp[t - 1] = __builtin_bit_cast(bf16x4, u);
      }
      u[0] = pack2(fmaf(-s01[0], cv0, av[t][0]) * s01[1], fmaf(-s01[2], cv0, av[t][1]) * s01[3]);
      u[1] = pack2(fmaf(-s23[0], cv0, av[t][2]) * s23[1], fmaf(-s23[2], cv0, av[t][3]) * s23[3]);
      Vp[t] = __builtin_bit_cast(bf16x4, u);
    }
    v2u O[2];
#pragma unroll
    for (int qt = 0; qt < 2; ++qt) {
      f32x4 bb = b0;
      if (qt == 0 && rt1 == 0 && rh == 0) bb = f32x4{-1.0e30f, -1.0e30f, -1.0e30f, -1.0e30f};
      const f32x4 S0 = mfma16(Kp[qt], Qp[qt], bb), S1 = mfma16(Kp[qt + 1], Qp[qt], b1);
      float m = fmaxf(fmaxf(fmaxf(S0[0], S0[1]), fmaxf(S0[2], S0[3])), fmaxf(fmaxf(S1[0], S1[1]), fmaxf(S1[2], S1[3])));
      m = xmax_g(m);
      constexpr float SC = 0.25f * 1.4426950408889634f;
      const float ms = -m * SC;
      float v[8];
#pragma unroll
      for (int i = 0; i < 4; ++i) { v[i] = __builtin_amdgcn_exp2f(fmaf(S0[i], SC, ms)); v[4 + i] = __builtin_amdgcn_exp2f(fmaf(S1[i], SC, ms)); }
      float sum = ((v[0] + v[1]) + (v[2] + v[3])) + ((v[4] + v[5]) + (v[6] + v[7]));
      sum = xsum_g(sum);
      const float inv = __builtin_amdgcn_rcpf(sum);
      v2u p0, p1;
      p0[0] = pack2(v[0] * inv, v[1] * inv); p0[1] = pack2(v[2] * inv, v[3] * inv);
      p1[0] = pack2(v[4] * inv, v[5] * inv); p1[1] = pack2(v[6] * inv, v[7] * inv);
      f32x4 o = mfma16(Vp[qt], __builtin_bit_cast(bf16x4, p0), z4);
      o = mfma16(Vp[qt + 1], __builtin_bit_cast(bf16x4, p1), o);
      O[qt][0] = pack2(o[0], o[1]); O[qt][1] = pack2(o[2], o[3]);
    }
    {
      const auto a = __builtin_amdgcn_permlane16_swap(O[0][0], O[1][0], false, false);
      const auto b = __builtin_amdgcn_permlane16_swap(O[0][1], O[1][1], false, false);
      const v4u o = {a[0], b[0], a[1], b[1]};
      const int head = 4 * hg + hh;
      const unsigned off = WS_ATT + ((4 * rt1 + 2 * rh + (g & 1)) * 16 + (head >> 1)) * 1024 + ((2 * (head & 1) + (g >> 1)) * 16 + r) * 16;
      WAITV(0);
      LC_ADD(10);
      __builtin_amdgcn_raw_buffer_store_b128(o, rws, off, 0, AUX_SC1);
    }
    STAMP(12);
    if (w < 4) {
#pragma unroll
      for (int j = 0; j < 4; ++j) xr[j] = *(const GAS v2u*)(p.x + (size_t)(64 * rt + 32 * mh + tk) * D_ + 64 * c8 + 32 * kh + 8 * j + 4 * hf);
      LC_WAITN(10, 8u);
      unsigned wb[2] = {wt[0] + WO_IMG + kh * 32768, wt[1] + WO_IMG + kh * 32768};
      asm volatile("" : "+v"(wb[0]), "+v"(wb[1]));
#pragma unroll
      for (int sx = 0; sx < 16; ++sx)
#pragma unroll
        for (int T = 0; T < 2; ++T) wf2[sx][T] = tr_frag((LAS unsigned char*)(size_t)wb[T] + (sx >> 1) * 4096 + (sx & 1) * 2048);
      LC_ADD(0);
    }
    WAITV(0);
    {
      unsigned old = 0;
      if (lane == 0) old = __hip_atomic_fetch_add(lc + 11, 1u, __ATOMIC_RELAXED, __HIP_MEMORY_SCOPE_WORKGROUP);
      old = __builtin_amdgcn_readfirstlane(old);
      if (old == 7u && lane == ((rt1 >> 2) & 1)) __hip_atomic_store(fl + (FL_F1 + rt1 * 128) / 4 + hg, tagv, RLX_AGENT);
    }
    STAMP(13);
  }
  const __amdgpu_buffer_rsrc_t rg = mkrs(p.wg, ZW3 ? 0 : D_ * F_ * 2), ru = mkrs(p.wu, ZW3 ? 0 : D_ * F_ * 2);
  const unsigned gl0 = wdma_lane(lane, F_ * 2, 0), gl1 = gl0 ^ 64;
#define HD 63
#define THR() asm volatile("s_waitcnt vmcnt(%0)" :: "n"(HD) : "memory")
  auto issue_gu2 = [&](int half, int ks, int dst) {
    const unsigned so = ((((8 * half) ^ fl3) | ((ks + r3) & 7)) * 32) * (F_ * 2) + fs * 128;
    dma16x4<8 * F_ * 2>(rg, dst + ks * 8192, gl0, gl1, so);
    THR();
    dma16x4<8 * F_ * 2>(ru, dst + ks * 8192 + 4096, gl1, gl0, so);
    THR();
  };
  auto issue_gu = [&](int half, int dst) {
    const unsigned so = ((((8 * half) ^ fl3) | ((w + r3) & 7)) * 32) * (F_ * 2) + fs * 128;
    dma16x4<8 * F_ * 2>(rg, dst + w * 8192, gl0, gl1, so);
    dma16x4<8 * F_ * 2>(ru, dst + w * 8192 + 4096, gl1, gl0, so);
  };
  const __amdgpu_buffer_rsrc_t rwd = mkrs(p.wd, ZW4 ? 0 : F_ * D_ * 2);
  const unsigned dl = wdma32_lane(lane, 1024);
  auto issue_wd4 = [&](int c, int qd) {
    const int pc_ = c + r4 >= 14 ? c + r4 - 14 : c + r4;
    dma16x4<8192>(rwd, wd_slot(c) + qd * 4096, dl, dl, (((qd >> 1) * 28 + 2 * pc_ + (qd & 1)) * 32) * 1024 + c8 * 128);
    THR();
  };
  auto issue_wd = [&](int c) {
    const int i = w * 2, half = i >> 3, ksl = (i >> 2) & 1, pk = i & 3;
    const int pc_ = c + r4 >= 14 ? c + r4 - 14 : c + r4;
    const unsigned so = ((half * 28 + 2 * pc_ + ksl) * 32 + 8 * pk) * 1024 + c8 * 128;
    dma16(rwd, wd_slot(c) + i * 1024, dl, so);
    dma16(rwd, wd_slot(c) + i * 1024 + 1024, dl, so + 8192);
  };
  unsigned hp[8];
  if (w >= 4) {
    if (has3) {
      LC_WAITN(11, 8u);
      issue_gu2(0, 2 * (w - 4), GU_A); issue_gu2(0, 2 * (w - 4) + 1, GU_A);
      while (__hip_atomic_load(lc, __ATOMIC_RELAXED, __HIP_MEMORY_SCOPE_WORKGROUP) < 4u) __builtin_amdgcn_s_sleep(1);
      issue_gu2(1, 2 * (w - 4), GU_B); issue_gu2(1, 2 * (w - 4) + 1, GU_B);
    } else {
      issue_wd4(1, w - 4); issue_wd4(2, w - 4); issue_wd4(3, w - 4); issue_wd4(4, w - 4);
    }
  } else {
    const int ct = c8;
    if (w == 0) {
      const int frt = lane < 8 ? rt : lane < 12 ? 4 * bx + (lane & 3) : 4 * pn + (lane & 3);
      const unsigned idx = (FL_F1 + frt * 128) / 4 + (lane < 8 ? lane : 0);
      const unsigned tg = __shfl(tagv, (frt >> 2) & 1);
      for (unsigned spins = 0;;) {
        const bool ok = lane >= 16 || __hip_atomic_load(fl + idx, RLX_AGENT) == tg;
        if (__all(ok)) break;
        if (poll_fail(spins, fl, lane)) break;
      }
      __builtin_amdgcn_fence(__ATOMIC_ACQUIRE, "wavefront");
      if (lane == 0) __hip_atomic_store(lc + 13, 1u, __ATOMIC_RELAXED, __HIP_MEMORY_SCOPE_WORKGROUP);
    } else {
      while (__hip_atomic_load(lc + 13, __ATOMIC_RELAXED, __HIP_MEMORY_SCOPE_WORKGROUP) == 0u) {}
      asm volatile("" ::: "memory");
    }
    STAMP(14);
    bf16x8 a[16];
    {
      const unsigned av_ = WS_ATT + ((4 * rt + 2 * mh + (tk >> 4)) * 16 + kh * 8) * 1024 + (hf * 16 + (tk & 15)) * 16;
#pragma unroll
      for (int sx = 0; sx < 16; ++sx)
        a[sx] = __builtin_bit_cast(bf16x8, __builtin_amdgcn_raw_buffer_load_b128(rwl, av_ + (sx & 1) * 512, (((sx >> 1) + r2) & 7) * 1024, AUX_SC1));
    }
    SB();
    f32x16 acc[2];
#pragma unroll
    for (int i = 0; i < 16; ++i) { acc[0][i] = 0.f; acc[1][i] = 0.f; }
#pragma unroll
    for (int sx = 0; sx < 16; ++sx)
#pragma unroll
      for (int T = 0; T < 2; ++T) acc[T] = __builtin_amdgcn_mfma_f32_32x32x16_bf16(wf2[sx][T], a[sx], acc[T], 0, 0, 0);
    STAMP(15);
    LAS f32x4* red = (LAS f32x4*)(L + RED);
#pragma unroll
    for (int j = 0; j < 4; ++j) {
      const f32x16& o = acc[kh ^ 1];
      red[(((mh * 2 + kh) * 4) + j) * 64 + lane] = f32x4{o[4 * j], o[4 * j + 1], o[4 * j + 2], o[4 * j + 3]};
    }
    WAITL0();
    if (lane == 0) __hip_atomic_fetch_add(lc + 2, 1u, __ATOMIC_RELAXED, __HIP_MEMORY_SCOPE_WORKGROUP);
    while (__hip_atomic_load(lc + 2, __ATOMIC_RELAXED, __HIP_MEMORY_SCOPE_WORKGROUP) < 4u) {}
    float s1 = 0.f, s2 = 0.f;
#pragma unroll
    for (int j = 0; j < 4; ++j) {
      const f32x4 o = red[(((mh * 2 + (kh ^ 1)) * 4) + j) * 64 + lane];
      const f32x16& m = acc[kh];
      const unsigned y01 = pack2(m[4 * j] + o[0], m[4 * j + 1] + o[1]), y23 = pack2(m[4 * j + 2] + o[2], m[4 * j + 3] + o[3]);
      hp[2 * j] = pack2(bflo(xr[j][0]) + bflo(y01), bfhi(xr[j][0]) + bfhi(y01));
      hp[2 * j + 1] = pack2(bflo(xr[j][1]) + bflo(y23), bfhi(xr[j][1]) + bfhi(y23));
      s1 = dot2(hp[2 * j], o2, s1); s1 = dot2(hp[2 * j + 1], o2, s1);
      s2 = dot2(hp[2 * j], hp[2 * j], s2); s2 = dot2(hp[2 * j + 1], hp[2 * j + 1], s2);
    }
    {
      const unsigned ho = WS_HF + ((4 * rt + 2 * mh + (tk >> 4)) * 16 + 2 * ct + kh) * 1024 + (hf * 16 + (tk & 15)) * 16;
#pragma unroll
      for (int jj = 0; jj < 2; ++jj) {
        const auto a0 = __builtin_amdgcn_permlane32_swap(hp[4 * jj], hp[4 * jj + 2], false, false);
        const auto a1 = __builtin_amdgcn_permlane32_swap(hp[4 * jj + 1], hp[4 * jj + 3], false, false);
        const v4u o = {a0[0], a1[0], a0[1], a1[1]};
        __builtin_amdgcn_raw_buffer_store_b128(o, rws, ho + jj * 512, 0, AUX_SC1);
      }
      asm volatile("s_nop 3" : "+v"(s1), "+v"(s2));
      float x0, x1;
      swap32(s1, x0, x1); s1 = x0 + x1;
      swap32(s2, x0, x1); s2 = x0 + x1;
      if (lane < 32) {
        v2u sv = {asu(s1), asu(s2)};
        __builtin_amdgcn_raw_buffer_store_b64(sv, rws, WS_ST + (((4 * rt + 2 * mh + (tk >> 4)) * 16 + 2 * ct + kh) * 16 + (tk & 15)) * 8, 0, AUX_SC1);
      }
    }
    STAMP(16);
    WAITV(0);
    if (lane == 0) {
      const unsigned old = __hip_atomic_fetch_add(lc + 1, 1u, __ATOMIC_RELAXED, __HIP_MEMORY_SCOPE_WORKGROUP);
      if (old == 3u) __hip_atomic_fetch_add(fl + (FL_C2 + rt * 128) / 4, 1u, RLX_AGENT);
    }
    STAMP(17);
  }
  WAITV(0);
  WAITL0();
  BAR();
  unsigned w4l[2], w4h[2], w4x[2];
  bf16x8 wr[16][2];
  auto wdfrag = [&](int sx, int T) {
    const int sl = wd_slot(sx >> 2), o = ((sx >> 1) & 1) * 4096 + (sx & 1) * 2048;
    return sl >= 131072 ? tr_frag((LAS unsigned char*)(size_t)w4x[T] + o)
         : sl >= 65536 ? tr_frag((LAS unsigned char*)(size_t)w4h[T] + (sl - 65536) + o) : tr_frag((LAS unsigned char*)(size_t)w4l[T] + sl + o);
  };
  auto p4_preread = [&]() {
#pragma unroll
    for (int T = 0; T < 2; ++T) {
      w4l[T] = wt[T] + kh * 8192; w4h[T] = w4l[T] + 65536; w4x[T] = w4l[T] + 131072;
      asm volatile("" : "+v"(w4l[T]), "+v"(w4h[T]), "+v"(w4x[T]));
    }
#pragma unroll
    for (int sx = 0; sx < 16; ++sx)
#pragma unroll
      for (int T = 0; T < 2; ++T) wr[sx][T] = wdfrag(sx, T);
    LC_ADD(3);
  };
  if (has3) {
    {
      f32x4 cs = z4;
      const unsigned wb = (wtr_lane(lane, w & 3) ^ ((w >> 2) << 6)) + (w >> 2) * 4096;
      bf16x8 t[16];
#pragma unroll
      for (int ks = 0; ks < 16; ++ks) t[ks] = tr_frag(L + ks * 8192 + wb);
      SB();
#pragma unroll
      for (int ks = 0; ks < 16; ++ks) cs = mfma32(t[ks], ones, cs);
      if (r == 0) *(LAS f32x4*)(L + P3_CS + (w * 16 + 4 * g) * 4) = cs;
    }
    STAMP(18);
    if (w == 0) {
      for (unsigned spins = 0;;) {
        const bool ok = lane >= 4 || __hip_atomic_load(fl + (FL_C2 + (4 * pn + (lane & 3)) * 128) / 4, RLX_AGENT) >= 8u;
        if (__all(ok)) break;
        if (poll_fail(spins, fl, lane)) break;
      }
      __builtin_amdgcn_fence(__ATOMIC_ACQUIRE, "wavefront");
      if (lane == 0) __hip_atomic_store(lc + 14, 1u, __ATOMIC_RELAXED, __HIP_MEMORY_SCOPE_WORKGROUP);
    } else {
      while (__hip_atomic_load(lc + 14, __ATOMIC_RELAXED, __HIP_MEMORY_SCOPE_WORKGROUP) == 0u) {}
      asm volatile("" ::: "memory");
    }
    STAMP(19);
    const int mt0 = 16 * pn + 2 * w, tk = lane & 31, hf = lane >> 5;
    bf16x8 af[32];
    v2u sp[8];
    const unsigned hsrc = WS_HF + (mt0 + (tk >> 4)) * 16384 + (hf * 16 + (tk & 15)) * 16;
    auto ldh = [&](int s) { af[s] = __builtin_bit_cast(bf16x8, __builtin_amdgcn_raw_buffer_load_b128(rwl, hsrc + (s & 1) * 512, ((((s >> 1) & 8) ^ fl3) | (((s >> 1) + r3) & 7)) * 1024, AUX_SC1)); };
    const unsigned ssrc = WS_ST + (((mt0 + (tk >> 4)) * 16 + 8 * hf) * 16 + (tk & 15)) * 8;
#pragma unroll
    for (int s = 0; s < 8; ++s) ldh(s);
    f32x16 acc[4];
#pragma unroll
    for (int n = 0; n < 4; ++n)
#pragma unroll
      for (int i = 0; i < 16; ++i) acc[n][i] = 0.f;
    unsigned wl[4], wlb[4];
    {
      const int q = (lane & 15) >> 2, pp = lane & 3, bb = (lane >> 4) & 1;
#pragma unroll
      for (int n = 0; n < 4; ++n) {
        const int pos = (2 * n + bb) ^ (((q >> 1) << 1) | (hf << 2) | 0) ^ ((pp >> 1) << 2);
        wl[n] = (pp >> 1) * 4096 + hf * 1024 + q * 128 + pos * 16 + (pp & 1) * 8;
        wlb[n] = wl[n] + GU_B;
        asm volatile("" : "+v"(wl[n]), "+v"(wlb[n]));
      }
    }
    auto rd = [&](unsigned base, int off) { return __builtin_amdgcn_ds_read_tr16_b64_v4i16((LAS bf16x4*)((LAS unsigned char*)(size_t)base + off)); };
#define WR3 6
    bf16x4 wh[WR3][2];
    auto rdM = [&](int M, int h) {
      const int n = M < 64 ? (M & 3) : (M - 64) >> 4, s = M < 64 ? (M >> 2) : ((M - 64) & 15);
      wh[M % WR3][h] = rd(M < 64 ? wl[n] : wlb[n], (s >> 1) * 8192 + (s & 1) * 2048 + h * 512);
    };
    auto mmM = [&](int M) {
      const int n = M < 64 ? (M & 3) : (M - 64) >> 4, s = M < 64 ? (M >> 2) : 16 + ((M - 64) & 15);
      acc[n] = __builtin_amdgcn_mfma_f32_32x32x16_bf16(__builtin_shufflevector(wh[M % WR3][0], wh[M % WR3][1], 0, 1, 2, 3, 4, 5, 6, 7), af[s], acc[n], 0, 0, 0);
    };
#pragma unroll
    for (int M = 0; M < WR3 - 1; ++M) { rdM(M, 0); rdM(M, 1); }
    SB();
    static_for<0, 64>([&](auto MM) {
      constexpr int M = decltype(MM)::value;
      mmM(M);
      rdM(M + WR3 - 1, 0); rdM(M + WR3 - 1, 1);
      if ((M & 1) == 0 && 8 + (M >> 1) < 32) ldh(8 + (M >> 1));
      if (M >= 48 && (M & 1) == 0) sp[(M - 48) >> 1] = __builtin_amdgcn_raw_buffer_load_b64(rwl, ssrc + (((M - 48) >> 1) & 7) * 128, 0, AUX_SC1);
      SB();
    });
    STAMP(20);
    float nmean, c1, c3, irs2;
    {
      float s1 = 0.f, s2 = 0.f;
#pragma unroll
      for (int i = 0; i < 8; ++i) { s1 += asf(sp[i][0]); s2 += asf(sp[i][1]); }
      float a, b;
      swap32(s1, a, b); s1 = a + b;
      swap32(s2, a, b); s2 = a + b;
      const float mn = s1 * (1.f / D_);
      irs2 = fmaxf(s2 * (1.f / D_) - mn * mn, 0.f) + LN_EPS;
      nmean = -mn;
      const float rs = __builtin_amdgcn_rsqf(irs2);
      c1 = -2.302208198f * rs; c3 = -0.1029432397f * rs * rs * rs;
      asm volatile("" : "+v"(nmean), "+v"(c1), "+v"(c3), "+v"(irs2));
    }
    WAITL0();
    BAR();
#pragma unroll
    for (int c = 0; c < 5; ++c) issue_wd(c);
    STAMP(21);
    SB();
    const unsigned actoff = WS_ACT + ((mt0 + (tk >> 4)) * 56 + 2 * fs) * 1024 + (hf * 16 + (tk & 15)) * 16;
    f32x4 cv[4];
    float eG[8], eT[8], eZ[8];
    unsigned ob[4];
    auto epi_step = [&](int n, int k) {
      const int st = k >> 3, v = k & 7, gi = (v >> 2) * 8 + (v & 3), ui = gi + 4;
      if (k == 0) {
        cv[0] = *(LAS f32x4*)(L + P3_CS + (n * 16 + 4 * hf) * 4); cv[1] = *(LAS f32x4*)(L + P3_CS + ((4 + n) * 16 + 4 * hf) * 4);
        cv[2] = *(LAS f32x4*)(L + P3_CS + (n * 16 + 8 + 4 * hf) * 4); cv[3] = *(LAS f32x4*)(L + P3_CS + ((4 + n) * 16 + 8 + 4 * hf) * 4);
      }
      else if (st == 1) eG[v] = fmaf(nmean, cv[(v >> 2) * 2][v & 3], acc[n][gi]);
      else if (st == 2) eT[v] = eG[v] * eG[v];
      else if (st == 3) eT[v] = fmaf(eT[v], c3, c1);
      else if (st == 4) eT[v] = eT[v] * eG[v];
      else if (st == 5) eT[v] = __builtin_amdgcn_exp2f(eT[v]);
      else if (st == 6) eZ[v] = fmaf(nmean, cv[(v >> 2) * 2 + 1][v & 3], acc[n][ui]);
      else if (st == 7) eT[v] = fmaf(eT[v], irs2, irs2);
      else if (st == 8) eZ[v] = eZ[v] * eG[v];
      else if (st == 9) eT[v] = __builtin_amdgcn_rcpf(eT[v]);
      else if (st == 10) eZ[v] = eZ[v] * eT[v];
      else if (k >= 88 && k < 92) ob[k - 88] = pack2(eZ[2 * (k - 88)], eZ[2 * (k - 88) + 1]);
      else if (k == 92) {
        const auto a0 = __builtin_amdgcn_permlane32_swap(ob[0], ob[2], false, false);
        const auto a1 = __builtin_amdgcn_permlane32_swap(ob[1], ob[3], false, false);
        const v4u ov = {a0[0], a1[0], a0[1], a1[1]};
        __builtin_amdgcn_raw_buffer_store_b128(ov, rws, actoff + (n >> 1) * 1024 + (n & 1) * 512, 0, AUX_SC1);
      }
    };
    static_for<64, 128>([&](auto MM) {
      constexpr int M = decltype(MM)::value;
      constexpr int n = (M - 64) >> 4, sl = (M - 64) & 15;
      mmM(M);
      if (M + WR3 - 1 < 128) { rdM(M + WR3 - 1, 0); rdM(M + WR3 - 1, 1); }
      if (n > 0) {
        if (sl == 0) epi_step(n - 1, 0);
        else if (sl <= 14) {
#pragma unroll
          for (int j = 0; j < 6; ++j) epi_step(n - 1, 8 + 6 * (sl - 1) + j);
        } else epi_step(n - 1, 92);
      }
      SB();
    });
    WAITV(3);
    LC_ADD(12);
    STAMP(22);
    epi_step(3, 0);
    static_for<8, 93>([&](auto KK) { epi_step(3, decltype(KK)::value); });
    STAMP(29);
    if (w < 4) { LC_WAITN(12, 8u); p4_preread(); }
    WAITV(0);
    BAR();
    if (tid < 8) __hip_atomic_fetch_add(fl + (FL_C3 + pn * 1024 + tid * 128) / 4, 1u, RLX_AGENT);
    STAMP(30);
  } else {
    issue_wd(0);
    WAITV(0);
    BAR();
    if (w < 4) p4_preread();
  }
  if (w >= 4) {
#pragma unroll
    for (int c = 5; c < 9; ++c) issue_wd4(c, w - 4);
    LC_WAIT(3);
#pragma unroll
    for (int c = 9; c < 13; ++c) issue_wd4(c, w - 4);
    WAITV(16);
    LC_ADD(4);
    WAITV(0);
    LC_ADD(9);
    LC_WAIT(5);
    issue_wd4(13, w - 4);
    WAITV(0);
    LC_ADD(6);
  } else {
    const int ct = c8;
    if (w == 0) {
      for (unsigned spins = 0;;) {
        const bool ok = __hip_atomic_load(fl + (FL_C3 + bx * 1024 + (bi & 7) * 128) / 4, RLX_AGENT) >= 28u;
        if (__all(ok)) break;
        if (poll_fail(spins, fl, lane)) break;
      }
      __builtin_amdgcn_fence(__ATOMIC_ACQUIRE, "wavefront");
      if (lane == 0) __hip_atomic_store(lc + 15, 1u, __ATOMIC_RELAXED, __HIP_MEMORY_SCOPE_WORKGROUP);
      if (bi == 31 && lane == (bx & 1)) __hip_atomic_store(fl + (FL_E + bx * 256) / 4, tagv, RLX_AGENT);
    } else {
      while (__hip_atomic_load(lc + 15, __ATOMIC_RELAXED, __HIP_MEMORY_SCOPE_WORKGROUP) == 0u) {}
      asm volatile("" ::: "memory");
    }
    STAMP(31);
    constexpr int AW = 10;
    bf16x8 a[AW];
    const unsigned asrc = WS_ACT + ((4 * rt + 2 * mh + (tk >> 4)) * 56 + kh * 28) * 1024 + (hf * 16 + (tk & 15)) * 16;
    auto lda = [&](int sx) { const int c = sx >> 2, pc_ = c + r4 >= 14 ? c + r4 - 14 : c + r4;
      a[sx % AW] = __builtin_bit_cast(bf16x8, __builtin_amdgcn_raw_buffer_load_b128(rwl, asrc + ((sx >> 1) & 1) * 1024 + (sx & 1) * 512, pc_ * 2048, AUX_SC1)); };
#pragma unroll
    for (int sx = 0; sx < AW; ++sx) lda(sx);
    f32x16 acc[2];
#pragma unroll
    for (int i = 0; i < 16; ++i) { acc[0][i] = 0.f; acc[1][i] = 0.f; }
#define WR4 4
    bf16x8 wf[WR4][2];
    SB();
    static_for<0, 56>([&](auto SS) {
      constexpr int sx = decltype(SS)::value;
      if (sx == 21 - WR4) LC_WAIT(4);
      if (sx == 37 - WR4) LC_WAIT(9);
      if (sx == 22 - WR4) LC_ADD(5);
      if (sx == 53 - WR4) LC_WAIT(6);
      if (sx + WR4 - 1 >= 16 && sx + WR4 - 1 < 56) { wf[(sx + WR4 - 1) % WR4][0] = wdfrag(sx + WR4 - 1, 0); wf[(sx + WR4 - 1) % WR4][1] = wdfrag(sx + WR4 - 1, 1); }
      acc[0] = __builtin_amdgcn_mfma_f32_32x32x16_bf16(sx < 16 ? wr[sx & 15][0] : wf[sx % WR4][0], a[sx % AW], acc[0], 0, 0, 0);
      acc[1] = __builtin_amdgcn_mfma_f32_32x32x16_bf16(sx < 16 ? wr[sx & 15][1] : wf[sx % WR4][1], a[sx % AW], acc[1], 0, 0, 0);
      if (sx + AW < 56) lda(sx + AW);
      SB();
    });
    STAMP(33);
    LAS f32x4* red = (LAS f32x4*)(L + SM - 16384);
    LC_ADD(7);
    LC_WAIT(7);
#pragma unroll
    for (int j = 0; j < 4; ++j) {
      const f32x16& o = acc[kh ^ 1];
      red[(((mh * 2 + kh) * 4) + j) * 64 + lane] = f32x4{o[4 * j], o[4 * j + 1], o[4 * j + 2], o[4 * j + 3]};
    }
    LC_ADD(8);
    LC_WAIT(8);
    unsigned op[8];
#pragma unroll
    for (int j = 0; j < 4; ++j) {
      const f32x4 o = red[(((mh * 2 + (kh ^ 1)) * 4) + j) * 64 + lane];
      const f32x16& m = acc[kh];
      const unsigned y01 = pack2(m[4 * j] + o[0], m[4 * j + 1] + o[1]), y23 = pack2(m[4 * j + 2] + o[2], m[4 * j + 3] + o[3]);
      op[2 * j] = pack2(bflo(hp[2 * j]) + bflo(y01), bfhi(hp[2 * j]) + bfhi(y01));
      op[2 * j + 1] = pack2(bflo(hp[2 * j + 1]) + bflo(y23), bfhi(hp[2 * j + 1]) + bfhi(y23));
    }
    unsigned short* orow = p.out + (size_t)(64 * rt + 32 * mh + tk) * D_ + 64 * ct + 32 * kh + 8 * hf;
#pragma unroll
    for (int jj = 0; jj < 2; ++jj) {
      const auto a0 = __builtin_amdgcn_permlane32_swap(op[4 * jj], op[4 * jj + 2], false, false);
      const auto a1 = __builtin_amdgcn_permlane32_swap(op[4 * jj + 1], op[4 * jj + 3], false, false);
      const v4u o = {a0[0], a1[0], a0[1], a1[1]};
      *(GAS v4u*)(orow + 16 * jj) = o;
    }
    STAMP(34);
  }
  STAMP_END();
}

#if defined(__HIP_DEVICE_COMPILE__)
#pragma clang attribute pop
#endif
extern "C" void kernel_launch(void* const* d_in, const int* in_sizes, int n_in, void* d_out, int out_size, void* d_ws, size_t ws_size, hipStream_t stream) {
  Params p;
  p.x = (const unsigned short*)d_in[0];
  p.wqkv = (const unsigned short*)d_in[1];
  p.wo = (const unsigned short*)d_in[2];
  p.wg = (const unsigned short*)d_in[4];
  p.wu = (const unsigned short*)d_in[5];
  p.wd = (const unsigned short*)d_in[6];
  p.out = (unsigned short*)d_out;
  p.ws = (unsigned char*)d_ws;
  constexpr int LDS = 160 * 1024;
  (void)hipFuncSetAttribute((const void*)k_fused, hipFuncAttributeMaxDynamicSharedMemorySize, LDS);
  k_fused<<<256, 512, LDS, stream>>>(p);
}
```

```cpp
#include <hip/hip_runtime.h>
#include <hip/hip_bf16.h>

#define GAS __attribute__((address_space(1)))
#define LAS __attribute__((address_space(3)))
typedef short bf16x8 __attribute__((ext_vector_type(8)));
typedef short bf16x4 __attribute__((ext_vector_type(4)));
typedef __bf16 bf2 __attribute__((ext_vector_type(2)));
typedef float f32x4 __attribute__((ext_vector_type(4)));
typedef float f32x2 __attribute__((ext_vector_type(2)));
typedef float f32x16 __attribute__((ext_vector_type(16)));
typedef unsigned v4u __attribute__((ext_vector_type(4)));
typedef unsigned v2u __attribute__((ext_vector_type(2)));
#define DI __device__ __forceinline__

constexpr int S_ = 2048, D_ = 512, F_ = 1792;
constexpr int AUX_SC1 = 16;
constexpr unsigned RSRC3 = 0x00020000u;
constexpr unsigned WS_ATT = 0u;
constexpr unsigned WS_HF = 2u << 20;
constexpr unsigned WS_ACT = 4u << 20;
constexpr unsigned WS_ST = 12u << 20;
constexpr unsigned WS_FL = 13u << 20;
constexpr unsigned FL_E = 0, FL_F1 = 4096, FL_C2 = 16384, FL_C3 = 24576, FL_TMO = 36864;
constexpr unsigned WS_BYTES = 14u << 20;
typedef GAS unsigned gu32;
#define RLX_AGENT __ATOMIC_RELAXED, __HIP_MEMORY_SCOPE_AGENT
constexpr unsigned SPIN_CAP = 1u << 18;

struct Params {
  const unsigned short* x;
  const unsigned short* wqkv;
  const unsigned short* wo;
  const unsigned short* wg;
  const unsigned short* wu;
  const unsigned short* wd;
  unsigned short* out;
  unsigned char* ws;
};

#if defined(__HIP_DEVICE_COMPILE__)
#pragma clang attribute push (__attribute__((target("no-packed-fp32-ops"))), apply_to = function)
#endif
template <int V> struct IC { static constexpr int value = V; };
template <int A, int B, class F> __device__ __forceinline__ void static_for(F&& f) { if constexpr (A < B) { f(IC<A>{}); static_for<A + 1, B>(f); } }
DI float asf(unsigned u) { return __builtin_bit_cast(float, u); }
DI unsigned asu(float f) { return __builtin_bit_cast(unsigned, f); }
DI f32x4 mfma32(bf16x8 a, bf16x8 b, f32x4 c) { return __builtin_amdgcn_mfma_f32_16x16x32_bf16(a, b, c, 0, 0, 0); }
DI f32x4 mfma16(bf16x4 a, bf16x4 b, f32x4 c) { return __builtin_amdgcn_mfma_f32_16x16x16bf16_1k(a, b, c, 0, 0, 0); }
DI unsigned pack2(float lo, float hi) { bf2 p; p[0] = (__bf16)lo; p[1] = (__bf16)hi; return __builtin_bit_cast(unsigned, p); }
DI float bflo(unsigned u) { return __builtin_bit_cast(float, u << 16); }
DI float bfhi(unsigned u) { return __builtin_bit_cast(float, u & 0xffff0000u); }
DI unsigned ones2() { unsigned o = 0x3F803F80u; asm volatile("" : "+v"(o)); return o; }
DI float dot2(unsigned a, unsigned b, float c) { return __builtin_amdgcn_fdot2_f32_bf16(__builtin_bit_cast(bf2, a), __builtin_bit_cast(bf2, b), c, false); }
DI __amdgpu_buffer_rsrc_t mkrs(const void* p, unsigned bytes) { return __builtin_amdgcn_make_buffer_rsrc((void*)p, 0, bytes, RSRC3); }
DI void dma16(__amdgpu_buffer_rsrc_t rs, unsigned dst, unsigned voff, unsigned soff) {
  asm volatile("s_mov_b32 m0, %0\n\ts_nop 0\n\tbuffer_load_dwordx4 %1, %2, %3 offen lds"
               :: "s"(dst), "v"(voff), "s"(rs), "s"(soff) : "m0", "memory");
}
template <int SS> DI void dma16x4(__amdgpu_buffer_rsrc_t rs, unsigned dst, unsigned v0, unsigned v1, unsigned soff) {
  unsigned t;
  asm volatile("s_mov_b32 m0, %1\n\ts_add_u32 %0, %5, %6\n\t"
               "buffer_load_dwordx4 %2, %4, %5 offen lds\n\t"
               "buffer_load_dwordx4 %3, %4, %0 offen offset:1024 lds\n\ts_add_u32 %0, %0, %6\n\t"
               "buffer_load_dwordx4 %2, %4, %0 offen offset:2048 lds\n\ts_add_u32 %0, %0, %6\n\t"
               "buffer_load_dwordx4 %3, %4, %0 offen offset:3072 lds"
               : "=&s"(t) : "s"(dst), "v"(v0), "v"(v1), "s"(rs), "s"(soff), "n"(SS - 1024) : "m0", "scc", "memory");
}
template <int LS> DI void dma16x3(__amdgpu_buffer_rsrc_t rs, unsigned dst, unsigned v, unsigned soff) {
  asm volatile("s_mov_b32 m0, %0\n\ts_nop 0\n\t"
               "buffer_load_dwordx4 %1, %2, %3 offen lds\n\ts_add_u32 m0, m0, %4\n\ts_nop 0\n\t"
               "buffer_load_dwordx4 %1, %2, %3 offen offset:1024 lds\n\ts_add_u32 m0, m0, %4\n\ts_nop 0\n\t"
               "buffer_load_dwordx4 %1, %2, %3 offen offset:2048 lds"
               :: "s"(dst), "v"(v), "s"(rs), "s"(soff), "n"(LS - 1024) : "m0", "scc", "memory");
}
DI bf16x8 lds_frag(LAS unsigned char* p) { return *(LAS bf16x8*)p; }
DI bf16x8 tr_frag(LAS unsigned char* p) {
  bf16x4 lo = __builtin_amdgcn_ds_read_tr16_b64_v4i16((LAS bf16x4*)p);
  bf16x4 hi = __builtin_amdgcn_ds_read_tr16_b64_v4i16((LAS bf16x4*)(p + 512));
  return __builtin_shufflevector(lo, hi, 0, 1, 2, 3, 4, 5, 6, 7);
}
DI void swap16(float v, float& a, float& b) {
  const unsigned u = asu(v);
  auto r = __builtin_amdgcn_permlane16_swap(u, u, false, false);
  a = asf(r[0]); b = asf(r[1]);
}
DI void swap32(float v, float& a, float& b) {
  const unsigned u = asu(v);
  auto r = __builtin_amdgcn_permlane32_swap(u, u, false, false);
  a = asf(r[0]); b = asf(r[1]);
}
DI float xsum_g(float v) {
  const f32x4 z = {0.f, 0.f, 0.f, 0.f};
  asm volatile("s_nop 3" : "+v"(v));
  return __builtin_amdgcn_mfma_f32_16x16x4f32(1.0f, v, z, 0, 0, 0)[0];
}
DI float xmax_g(float v) {
  float a, b;
  swap16(v, a, b); v = fmaxf(a, b);
  swap32(v, a, b); return fmaxf(a, b);
}
#define WAITV(n) asm volatile("s_waitcnt vmcnt(" #n ")" ::: "memory")
#define WAITL0() asm volatile("s_waitcnt lgkmcnt(0)" ::: "memory")
#define BAR() asm volatile("s_barrier" ::: "memory")
#define SB() __builtin_amdgcn_sched_barrier(0)
#define SG(mask, n) __builtin_amdgcn_sched_group_barrier(mask, n, 0)
#define M_MFMA 0x8
#define M_DSR 0x100
#define M_DSW 0x200
#define M_VALU 0x2
#define M_TRANS 0x400
#define M_VMEMR 0x20
constexpr float LN_EPS = 1e-5f;
#define STAMP_DECL do {} while (0)
#define STAMP(i) do {} while (0)
#define STAMP_END() do {} while (0)

DI unsigned wtr_lane(int lane, int nt) {
  const int g = lane >> 4, q = (lane & 15) >> 2, p = lane & 3;
  const int sg = (q >> 1) | ((g & 1) << 1);
  return g * 1024 + q * 128 + ((nt ^ sg) << 5) + p * 8;
}
DI unsigned wdma32_lane(int lane, unsigned pitch) {
  const int r8 = lane >> 3, pos = lane & 7;
  return r8 * pitch + ((pos ^ (((r8 >> 1) & 1) << 2)) << 4);
}
DI unsigned wdma_lane(int lane, unsigned pitch, int odd) {
  const int r8 = lane >> 3, pos = lane & 7;
  return r8 * pitch + ((pos ^ (((r8 >> 1) & 1) << 1) ^ (odd << 2)) << 4);
}

#define PSLEEP 1
#define ZW1 0
#define ZW2 0
#define ZW3 0
#define ZW4 0
#define ZX 0
#define ZA 0
constexpr int P1_X = 0;
constexpr int P1_W = 81920;
constexpr int SM = 155648;
constexpr int P1_PART = SM;
constexpr int P1_CS = SM + 2560;
constexpr int P1_FIN = SM + 4096;
constexpr int LC = SM + 7168;
constexpr int P3_CS = SM;
constexpr int WO_IMG = 65536;
constexpr int RED = 131072;
constexpr int GU_A = 0;
constexpr int GU_B = 65536;
DI constexpr int wd_slot(int c) { return (c % 9) == 0 ? 131072 : ((c % 9) - 1) * 16384; }

DI bool poll_fail(unsigned& spins, gu32* fl, int lane) {
  __builtin_amdgcn_s_sleep(PSLEEP);
  if (++spins > SPIN_CAP) { if (lane == 0) __hip_atomic_store(fl + FL_TMO / 4, 1u, RLX_AGENT); return true; }
  return false;
}

__global__ __launch_bounds__(512, 2) void k_fused(Params p) {
  extern __shared__ __attribute__((aligned(16))) unsigned char smem[];
  LAS unsigned char* L = (LAS unsigned char*)smem;
  const int tid = threadIdx.x, lane = tid & 63, w = __builtin_amdgcn_readfirstlane(tid >> 6);
  const int bx = blockIdx.x & 7, bi = blockIdx.x >> 3, rt = 4 * bx + (bi >> 3), c8 = bi & 7;
  const int pr = bx >> 1, pc = bx & 1;
  const int r = lane & 15, g = lane >> 4;
  const __amdgpu_buffer_rsrc_t rws = mkrs(p.ws, WS_BYTES);
  const __amdgpu_buffer_rsrc_t rwl = mkrs(p.ws, ZA ? 0 : WS_BYTES);
  gu32* fl = (gu32*)(p.ws + WS_FL);
  const f32x4 z4 = {0.f, 0.f, 0.f, 0.f};
  const bf16x8 ones = {0x3F80, 0x3F80, 0x3F80, 0x3F80, 0x3F80, 0x3F80, 0x3F80, 0x3F80};
  const unsigned o2 = ones2();
  const int rt1 = 8 * pr + (bi >> 2), hg = 4 * pc + (bi & 3);
  const bool has3 = bi < 28;
  const int pn = 2 * pr + (bi >= 14 ? 1 : 0), fs = 14 * pc + (bi >= 14 ? bi - 14 : bi);
  const int r2 = (c8 + 2 * (rt & 3)) & 7, r3 = fs & 7, fl3 = bi >= 14 ? 8 : 0, r4 = (2 * c8 + 3 * (rt & 3)) % 14;
  if (tid < 16) ((LAS unsigned*)(L + LC))[tid] = 0u;
  STAMP_DECL;
  STAMP(0);
  const unsigned tagv = __hip_atomic_load(fl + (FL_E + (2 * pr + (lane & 1)) * 256) / 4, RLX_AGENT) + 1u;
  if (w == 0) {
    if (hg == 0 && lane == 0) {
      __hip_atomic_store(fl + (FL_C2 + rt1 * 128) / 4, 0u, RLX_AGENT);
      if ((rt1 & 3) == 0) { for (int i = 0; i < 8; ++i) __hip_atomic_store(fl + (FL_C3 + (rt1 >> 2) * 1024 + i * 128) / 4, 0u, RLX_AGENT); }
    }
  }
#define LC_ADD(i) do { WAITL0(); if (lane == 0) __hip_atomic_fetch_add(lc + (i), 1u, __ATOMIC_RELAXED, __HIP_MEMORY_SCOPE_WORKGROUP); } while (0)
#define LC_WAITN(i, n) do { while (__hip_atomic_load(lc + (i), __ATOMIC_RELAXED, __HIP_MEMORY_SCOPE_WORKGROUP) < (n)) __builtin_amdgcn_s_sleep(1); asm volatile("" ::: "memory"); } while (0)
#define LC_WAIT(i) LC_WAITN(i, 4u)
  LAS unsigned* lc = (LAS unsigned*)(L + LC);
  const int tk = lane & 31, hf = lane >> 5, mh = w & 1, kh = (w >> 1) & 1;
  unsigned wt[2];
  {
    const int q = (lane & 15) >> 2, pp = lane & 3, bb = (lane >> 4) & 1;
#pragma unroll
    for (int T = 0; T < 2; ++T) wt[T] = hf * 1024 + q * 128 + (((4 * T + 2 * bb + (pp >> 1)) ^ ((q >> 1) << 2)) << 4) + (pp & 1) * 8;
  }
  bf16x8 wf2[16][2];
  v2u xr[4];
  unsigned tl_ = 0;
  if (w == 1) {
    const GAS unsigned* tp = lane < 56 ? (const GAS unsigned*)(p.ws + (size_t)lane * 262144) : (const GAS unsigned*)((const unsigned char*)p.out + (size_t)(lane - 56) * 262144);
    asm volatile("global_load_dword %0, %1, off sc1" : "=&v"(tl_) : "v"(tp) : "memory");
  }
  {
    const int hh = w & 3, rh = w >> 2, rot = (bi + (bi >> 2) + 2 * pr) & 7;
    const __amdgpu_buffer_rsrc_t rx = mkrs(p.x, ZX ? 0 : S_ * D_ * 2), rw = mkrs(p.wqkv, ZW1 ? 0 : D_ * 3 * D_ * 2);
    const int r8 = lane >> 3, pos = lane & 7;
    const unsigned xv0 = r8 * 1024 + ((pos ^ (r8 >> 1)) << 4), xv1 = xv0 ^ 64;
    auto issue_x = [&](int half) {
      const int c = 4 * half + (w >> 1);
#pragma unroll
      for (int i = 0; i < 5; ++i) {
        const int P = 5 * (w & 1) + i;
        int row = 64 * rt1 - 16 + 8 * P;
        row = row < 0 ? 0 : row;
        dma16(rx, P1_X + (c * 10 + P) * 1024, (P & 1) ? xv1 : xv0, row * 1024 + ((c + rot) & 7) * 128);
      }
    };
    const unsigned wv = wdma_lane(lane, 3072, w & 1);
    auto issue_w = [&](int c) {
      dma16x3<8192>(rw, P1_W + (c % 3) * 24576 + w * 1024, wv, (((c + rot) & 7) * 64 + 8 * w) * 3072 + hg * 128);
    };
    issue_x(0); issue_w(0); issue_w(1);
    STAMP(1);
    f32x4 aq[2] = {z4, z4}, ak[3] = {z4, z4, z4}, av[3] = {z4, z4, z4}, aqh = z4;
    float s1 = 0.f, s2 = 0.f;
    const unsigned xlane = (r >> 3) * 1024 + (r & 7) * 128 + ((g ^ ((r >> 1) & 7)) << 4);
    unsigned xl0 = xlane + 2 * rh * 2048, xl1 = xl0 ^ 64;
    const int stile = rh == 0 ? (hh < 3 ? hh : 0) : (hh < 2 ? 3 + hh : 3);
    unsigned xs0 = xlane + stile * 2048, xs1 = xs0 ^ 64;
    unsigned wlA = wtr_lane(lane, hh) + P1_W, wlB = wlA + 49152;
    unsigned xh0 = xl0 + 40960, xh1 = xl1 + 40960, xsh0 = xs0 + 40960, xsh1 = xs1 + 40960;
    asm volatile("" : "+v"(xl0), "+v"(xl1), "+v"(xs0), "+v"(xs1), "+v"(wlA), "+v"(wlB), "+v"(xh0), "+v"(xh1), "+v"(xsh0), "+v"(xsh1));
    bf16x8 fx[2][4];
    bf16x4 fw[2][3][2];
    auto rd1 = [&](int K, int j) {
      const int c = K >> 1, sx = K & 1, bf = K & 1;
      if (j < 6) {
        const int part = j >> 1, h = j & 1;
        const unsigned base = (c % 3) == 2 ? wlB : wlA;
        const int off = ((c % 3) == 2 ? 0 : (c % 3) * 24576) + part * 8192 + sx * 4096 + h * 512;
        fw[bf][part][h] = __builtin_amdgcn_ds_read_tr16_b64_v4i16((LAS bf16x4*)((LAS unsigned char*)(size_t)base + off));
      } else if (j < 9) {
        const int t = j == 6 ? 1 : j == 7 ? 2 : 0;
        const unsigned base = c < 4 ? (sx ? xl1 : xl0) : (sx ? xh1 : xh0);
        fx[bf][t] = lds_frag((LAS unsigned char*)(size_t)base + (c & 3) * 10240 + t * 2048);
      } else {
        const unsigned base = c < 4 ? (sx ? xs1 : xs0) : (sx ? xsh1 : xsh0);
        fx[bf][3] = lds_frag((LAS unsigned char*)(size_t)base + (c & 3) * 10240);
      }
    };
    constexpr int RORD[10] = {0, 1, 6, 7, 2, 3, 8, 4, 5, 9};
    auto wfrag = [&](int bf, int part) { return __builtin_shufflevector(fw[bf][part][0], fw[bf][part][1], 0, 1, 2, 3, 4, 5, 6, 7); };
    auto mm1 = [&](int K, int j) {
      const int bf = K & 1;
      if (j == 0) aq[0] = mfma32(wfrag(bf, 0), fx[bf][1], aq[0]);
      else if (j == 1) aq[1] = mfma32(wfrag(bf, 0), fx[bf][2], aq[1]);
      else if (j == 2) ak[1] = mfma32(wfrag(bf, 1), fx[bf][1], ak[1]);
      else if (j == 3) ak[2] = mfma32(wfrag(bf, 1), fx[bf][2], ak[2]);
      else if (j == 4) ak[0] = mfma32(wfrag(bf, 1), fx[bf][0], ak[0]);
      else if (j == 5) av[0] = mfma32(fx[bf][0], wfrag(bf, 2), av[0]);
      else if (j == 6) av[1] = mfma32(fx[bf][1], wfrag(bf, 2), av[1]);
      else av[2] = mfma32(fx[bf][2], wfrag(bf, 2), av[2]);
    };
    auto ones_row = [&](int half) {
      if ((w & 1) == 0 && lane < 8) *(LAS v4u*)(L + P1_X + (4 * half + (w >> 1)) * 10240 + lane * 16) = v4u{0x3F803F80u, 0x3F803F80u, 0x3F803F80u, 0x3F803F80u};
    };
    f32x4 b0, b1;
#pragma unroll
    for (int i = 0; i < 4; ++i) {
      const int d0 = 16 + r - (4 * g + i), d1 = r - (4 * g + i);
      b0[i] = (d0 <= 14 && !(d0 & 1)) ? 0.f : -1.0e30f;
      b1[i] = (d1 >= 0 && d1 <= 14 && !(d1 & 1)) ? 0.f : -1.0e30f;
    }
    asm volatile("" : "+v"(b0), "+v"(b1));
    WAITV(3);
    asm volatile("" :: "v"(tl_));
    ones_row(0);
    WAITL0();
    BAR();
    STAMP(2);
    issue_w(2);
    v4u wu_[3];
    {
      const unsigned b = blockIdx.x;
      const GAS v4u* q0 = w < 7 ? (const GAS v4u*)p.wg + b * 448 + w * 64 + lane : (const GAS v4u*)p.wo + b * 128 + lane;
      const GAS v4u* q1 = w < 7 ? (const GAS v4u*)p.wu + b * 448 + w * 64 + lane : (const GAS v4u*)p.wo + b * 128 + 64 + lane;
      const GAS v4u* q2 = (const GAS v4u*)p.wd + b * 448 + (w < 7 ? w : 6) * 64 + lane;
      asm volatile("global_load_dwordx4 %0, %3, off\n\tglobal_load_dwordx4 %1, %4, off\n\tglobal_load_dwordx4 %2, %5, off"
                   : "=&v"(wu_[0]), "=&v"(wu_[1]), "=&v"(wu_[2]) : "v"(q0), "v"(q1), "v"(q2) : "memory");
    }
#pragma unroll
    for (int j = 0; j < 10; ++j) rd1(0, RORD[j]);
    SB();
    static_for<0, 16>([&](auto KK) {
      constexpr int K = decltype(KK)::value, c = K >> 1;
      if constexpr ((K & 1) == 1 && c < 7) {
        if (c == 0) WAITV(6);
        else if (c == 1) WAITV(11);
        else if (c == 2) WAITV(8);
        else if (c == 6) WAITV(0);
        else WAITV(3);
        if (c == 2) asm volatile("" :: "v"(wu_[0]), "v"(wu_[1]), "v"(wu_[2]));
        if (c == 3) ones_row(1);
        WAITL0();
        BAR();
        STAMP(3 + c);
        if (c + 3 < 8) issue_w(c + 3);
        if (c == 0) issue_x(1);
        SB();
      }
      static_for<0, 8>([&](auto JJ) {
        constexpr int j = decltype(JJ)::value;
        mm1(K, j);
        if (K + 1 < 16) { rd1(K + 1, RORD[j]); if (j >= 6) rd1(K + 1, RORD[j + 2]); }
        { const v4u u = __builtin_bit_cast(v4u, fx[K & 1][3]);
          if (j & 1) s2 = dot2(u[j >> 1], u[j >> 1], s2); else s1 = dot2(u[j >> 1], o2, s1); }
        SB();
      });
      if (rh == 0) aqh = mfma32(wfrag(K & 1, 0), fx[K & 1][0], aqh);
      SB();
    });
    STAMP(10);
    LAS f32x2* fin = (LAS f32x2*)(L + P1_FIN);
    LAS float* csx = (LAS float*)(L + P1_CS);
    {
      const float a = xsum_g(s1), b = xsum_g(s2);
      const float mean = a * (1.f / D_);
      const float rstd = __builtin_amdgcn_rsqf(fmaxf(b * (1.f / D_) - mean * mean, 0.f) + LN_EPS);
      if (lane < 16 && (rh == 0 ? hh < 3 : hh < 2)) fin[stile * 16 + lane] = f32x2{mean, rstd};
      if (rh == 0) {
        if (r == 0) { *(LAS f32x4*)&csx[hh * 48 + 4 * g] = aqh; *(LAS f32x4*)&csx[hh * 48 + 16 + 4 * g] = ak[0]; }
        if (lane < 16) csx[hh * 48 + 32 + lane] = av[0][0];
      }
    }
    WAITL0();
    BAR();
    {
      const __amdgpu_buffer_rsrc_t rwo = mkrs(p.wo, ZW2 ? 0 : D_ * D_ * 2);
      const unsigned a0 = wdma32_lane(lane, 1024), a1 = a0;
      dma16x4<8192>(rwo, WO_IMG + w * 8192, a0, a1, (((2 * w) & 8) | ((2 * w + r2) & 7)) * 32768 + c8 * 128);
      dma16x4<8192>(rwo, WO_IMG + w * 8192 + 4096, a0, a1, (((2 * w) & 8) | ((2 * w + 1 + r2) & 7)) * 32768 + c8 * 128);
    }
    const f32x4 cq = *(LAS f32x4*)&csx[hh * 48 + 4 * g], ck = *(LAS f32x4*)&csx[hh * 48 + 16 + 4 * g];
    const float cv0 = csx[hh * 48 + 32 + r];
    STAMP(11);
    bf16x4 Qp[2], Kp[3], Vp[3];
#pragma unroll
    for (int t = 0; t < 3; ++t) {
      const f32x2 st = fin[(2 * rh + t) * 16 + r];
      const f32x4 s01 = *(LAS f32x4*)&fin[(2 * rh + t) * 16 + 4 * g], s23 = *(LAS f32x4*)&fin[(2 * rh + t) * 16 + 4 * g + 2];
      const float nm = -st[0], rs = st[1];
      v2u u;
      u[0] = pack2(fmaf(nm, ck[0], ak[t][0]) * rs, fmaf(nm, ck[1], ak[t][1]) * rs);
      u[1] = pack2(fmaf(nm, ck[2], ak[t][2]) * rs, fmaf(nm, ck[3], ak[t][3]) * rs);
      Kp[t] = __builtin_bit_cast(bf16x4, u);
      if (t >= 1) {
        u[0] = pack2(fmaf(nm, cq[0], aq[t - 1][0]) * rs, fmaf(nm, cq[1], aq[t - 1][1]) * rs);
        u[1] = pack2(fmaf(nm, cq[2], aq[t - 1][2]) * rs, fmaf(nm, cq[3], aq[t - 1][3]) * rs);
        Qp[t - 1] = __builtin_bit_cast(bf16x4, u);
      }
      u[0] = pack2(fmaf(-s01[0], cv0, av[t][0]) * s01[1], fmaf(-s01[2], cv0, av[t][1]) * s01[3]);
      u[1] = pack2(fmaf(-s23[0], cv0, av[t][2]) * s23[1], fmaf(-s23[2], cv0, av[t][3]) * s23[3]);
      Vp[t] = __builtin_bit_cast(bf16x4, u);
    }
    v2u O[2];
#pragma unroll
    for (int qt = 0; qt < 2; ++qt) {
      f32x4 bb = b0;
      if (qt == 0 && rt1 == 0 && rh == 0) bb = f32x4{-1.0e30f, -1.0e30f, -1.0e30f, -1.0e30f};
      const f32x4 S0 = mfma16(Kp[qt], Qp[qt], bb), S1 = mfma16(Kp[qt + 1], Qp[qt], b1);
      float m = fmaxf(fmaxf(fmaxf(S0[0], S0[1]), fmaxf(S0[2], S0[3])), fmaxf(fmaxf(S1[0], S1[1]), fmaxf(S1[2], S1[3])));
      m = xmax_g(m);
      constexpr float SC = 0.25f * 1.4426950408889634f;
      const float ms = -m * SC;
      float v[8];
#pragma unroll
      for (int i = 0; i < 4; ++i) { v[i] = __builtin_amdgcn_exp2f(fmaf(S0[i], SC, ms)); v[4 + i] = __builtin_amdgcn_exp2f(fmaf(S1[i], SC, ms)); }
      float sum = ((v[0] + v[1]) + (v[2] + v[3])) + ((v[4] + v[5]) + (v[6] + v[7]));
      sum = xsum_g(sum);
      const float inv = __builtin_amdgcn_rcpf(sum);
      v2u p0, p1;
      p0[0] = pack2(v[0] * inv, v[1] * inv); p0[1] = pack2(v[2] * inv, v[3] * inv);
      p1[0] = pack2(v[4] * inv, v[5] * inv); p1[1] = pack2(v[6] * inv, v[7] * inv);
      f32x4 o = mfma16(Vp[qt], __builtin_bit_cast(bf16x4, p0), z4);
      o = mfma16(Vp[qt + 1], __builtin_bit_cast(bf16x4, p1), o);
      O[qt][0] = pack2(o[0], o[1]); O[qt][1] = pack2(o[2], o[3]);
    }
    {
      const auto a = __builtin_amdgcn_permlane16_swap(O[0][0], O[1][0], false, false);
      const auto b = __builtin_amdgcn_permlane16_swap(O[0][1], O[1][1], false, false);
      const v4u o = {a[0], b[0], a[1], b[1]};
      const int head = 4 * hg + hh;
      const unsigned off = WS_ATT + ((4 * rt1 + 2 * rh + (g & 1)) * 16 + (head >> 1)) * 1024 + ((2 * (head & 1) + (g >> 1)) * 16 + r) * 16;
      WAITV(0);
      LC_ADD(10);
      __builtin_amdgcn_raw_buffer_store_b128(o, rws, off, 0, AUX_SC1);
    }
    STAMP(12);
    if (w < 4) {
#pragma unroll
      for (int j = 0; j < 4; ++j) xr[j] = *(const GAS v2u*)(p.x + (size_t)(64 * rt + 32 * mh + tk) * D_ + 64 * c8 + 32 * kh + 8 * j + 4 * hf);
      LC_WAITN(10, 8u);
      unsigned wb[2] = {wt[0] + WO_IMG + kh * 32768, wt[1] + WO_IMG + kh * 32768};
      asm volatile("" : "+v"(wb[0]), "+v"(wb[1]));
#pragma unroll
      for (int sx = 0; sx < 16; ++sx)
#pragma unroll
        for (int T = 0; T < 2; ++T) wf2[sx][T] = tr_frag((LAS unsigned char*)(size_t)wb[T] + (sx >> 1) * 4096 + (sx & 1) * 2048);
      LC_ADD(0);
    }
    WAITV(0);
    {
      unsigned old = 0;
      if (lane == 0) old = __hip_atomic_fetch_add(lc + 11, 1u, __ATOMIC_RELAXED, __HIP_MEMORY_SCOPE_WORKGROUP);
      old = __builtin_amdgcn_readfirstlane(old);
      if (old == 7u && lane == ((rt1 >> 2) & 1)) __hip_atomic_store(fl + (FL_F1 + rt1 * 128) / 4 + hg, tagv, RLX_AGENT);
    }
    STAMP(13);
  }
  const __amdgpu_buffer_rsrc_t rg = mkrs(p.wg, ZW3 ? 0 : D_ * F_ * 2), ru = mkrs(p.wu, ZW3 ? 0 : D_ * F_ * 2);
  const unsigned gl0 = wdma_lane(lane, F_ * 2, 0), gl1 = gl0 ^ 64;
#define HD 63
#define THR() asm volatile("s_waitcnt vmcnt(%0)" :: "n"(HD) : "memory")
  auto issue_gu2 = [&](int half, int ks, int dst) {
    const unsigned so = ((((8 * half) ^ fl3) | ((ks + r3) & 7)) * 32) * (F_ * 2) + fs * 128;
    dma16x4<8 * F_ * 2>(rg, dst + ks * 8192, gl0, gl1, so);
    THR();
    dma16x4<8 * F_ * 2>(ru, dst + ks * 8192 + 4096, gl1, gl0, so);
    THR();
  };
  auto issue_gu = [&](int half, int dst) {
    const unsigned so = ((((8 * half) ^ fl3) | ((w + r3) & 7)) * 32) * (F_ * 2) + fs * 128;
    dma16x4<8 * F_ * 2>(rg, dst + w * 8192, gl0, gl1, so);
    dma16x4<8 * F_ * 2>(ru, dst + w * 8192 + 4096, gl1, gl0, so);
  };
  const __amdgpu_buffer_rsrc_t rwd = mkrs(p.wd, ZW4 ? 0 : F_ * D_ * 2);
  const unsigned dl = wdma32_lane(lane, 1024);
  auto issue_wd4 = [&](int c, int qd) {
    const int pc_ = c + r4 >= 14 ? c + r4 - 14 : c + r4;
    dma16x4<8192>(rwd, wd_slot(c) + qd * 4096, dl, dl, (((qd >> 1) * 28 + 2 * pc_ + (qd & 1)) * 32) * 1024 + c8 * 128);
    THR();
  };
  auto issue_wd = [&](int c) {
    const int i = w * 2, half = i >> 3, ksl = (i >> 2) & 1, pk = i & 3;
    const int pc_ = c + r4 >= 14 ? c + r4 - 14 : c + r4;
    const unsigned so = ((half * 28 + 2 * pc_ + ksl) * 32 + 8 * pk) * 1024 + c8 * 128;
    dma16(rwd, wd_slot(c) + i * 1024, dl, so);
    dma16(rwd, wd_slot(c) + i * 1024 + 1024, dl, so + 8192);
  };
  unsigned hp[8];
  if (w >= 4) {
    if (has3) {
      LC_WAITN(11, 8u);
      issue_gu2(0, 2 * (w - 4), GU_A); issue_gu2(0, 2 * (w - 4) + 1, GU_A);
      while (__hip_atomic_load(lc, __ATOMIC_RELAXED, __HIP_MEMORY_SCOPE_WORKGROUP) < 4u) __builtin_amdgcn_s_sleep(1);
      issue_gu2(1, 2 * (w - 4), GU_B); issue_gu2(1, 2 * (w - 4) + 1, GU_B);
    } else {
      issue_wd4(1, w - 4); issue_wd4(2, w - 4); issue_wd4(3, w - 4); issue_wd4(4, w - 4);
    }
  } else {
    const int ct = c8;
    if (w == 0) {
      const int frt = lane < 8 ? rt : lane < 12 ? 4 * bx + (lane & 3) : 4 * pn + (lane & 3);
      const unsigned idx = (FL_F1 + frt * 128) / 4 + (lane < 8 ? lane : 0);
      const unsigned tg = __shfl(tagv, (frt >> 2) & 1);
      for (unsigned spins = 0;;) {
        const bool ok = lane >= 16 || __hip_atomic_load(fl + idx, RLX_AGENT) == tg;
        if (__all(ok)) break;
        if (poll_fail(spins, fl, lane)) break;
      }
      __builtin_amdgcn_fence(__ATOMIC_ACQUIRE, "wavefront");
      if (lane == 0) __hip_atomic_store(lc + 13, 1u, __ATOMIC_RELAXED, __HIP_MEMORY_SCOPE_WORKGROUP);
    } else {
      while (__hip_atomic_load(lc + 13, __ATOMIC_RELAXED, __HIP_MEMORY_SCOPE_WORKGROUP) == 0u) {}
      asm volatile("" ::: "memory");
    }
    STAMP(14);
    bf16x8 a[16];
    {
      const unsigned av_ = WS_ATT + ((4 * rt + 2 * mh + (tk >> 4)) * 16 + kh * 8) * 1024 + (hf * 16 + (tk & 15)) * 16;
#pragma unroll
      for (int sx = 0; sx < 16; ++sx)
        a[sx] = __builtin_bit_cast(bf16x8, __builtin_amdgcn_raw_buffer_load_b128(rwl, av_ + (sx & 1) * 512, (((sx >> 1) + r2) & 7) * 1024, AUX_SC1));
    }
    SB();
    f32x16 acc[2];
#pragma unroll
    for (int i = 0; i < 16; ++i) { acc[0][i] = 0.f; acc[1][i] = 0.f; }
#pragma unroll
    for (int sx = 0; sx < 16; ++sx)
#pragma unroll
      for (int T = 0; T < 2; ++T) acc[T] = __builtin_amdgcn_mfma_f32_32x32x16_bf16(wf2[sx][T], a[sx], acc[T], 0, 0, 0);
    STAMP(15);
    LAS f32x4* red = (LAS f32x4*)(L + RED);
#pragma unroll
    for (int j = 0; j < 4; ++j) {
      const f32x16& o = acc[kh ^ 1];
      red[(((mh * 2 + kh) * 4) + j) * 64 + lane] = f32x4{o[4 * j], o[4 * j + 1], o[4 * j + 2], o[4 * j + 3]};
    }
    WAITL0();
    if (lane == 0) __hip_atomic_fetch_add(lc + 2, 1u, __ATOMIC_RELAXED, __HIP_MEMORY_SCOPE_WORKGROUP);
    while (__hip_atomic_load(lc + 2, __ATOMIC_RELAXED, __HIP_MEMORY_SCOPE_WORKGROUP) < 4u) {}
    float s1 = 0.f, s2 = 0.f;
#pragma unroll
    for (int j = 0; j < 4; ++j) {
      const f32x4 o = red[(((mh * 2 + (kh ^ 1)) * 4) + j) * 64 + lane];
      const f32x16& m = acc[kh];
      const unsigned y01 = pack2(m[4 * j] + o[0], m[4 * j + 1] + o[1]), y23 = pack2(m[4 * j + 2] + o[2], m[4 * j + 3] + o[3]);
      hp[2 * j] = pack2(bflo(xr[j][0]) + bflo(y01), bfhi(xr[j][0]) + bfhi(y01));
      hp[2 * j + 1] = pack2(bflo(xr[j][1]) + bflo(y23), bfhi(xr[j][1]) + bfhi(y23));
      s1 = dot2(hp[2 * j], o2, s1); s1 = dot2(hp[2 * j + 1], o2, s1);
      s2 = dot2(hp[2 * j], hp[2 * j], s2); s2 = dot2(hp[2 * j + 1], hp[2 * j + 1], s2);
    }
    {
      const unsigned ho = WS_HF + ((4 * rt + 2 * mh + (tk >> 4)) * 16 + 2 * ct + kh) * 1024 + (hf * 16 + (tk & 15)) * 16;
#pragma unroll
      for (int jj = 0; jj < 2; ++jj) {
        const auto a0 = __builtin_amdgcn_permlane32_swap(hp[4 * jj], hp[4 * jj + 2], false, false);
        const auto a1 = __builtin_amdgcn_permlane32_swap(hp[4 * jj + 1], hp[4 * jj + 3], false, false);
        const v4u o = {a0[0], a1[0], a0[1], a1[1]};
        __builtin_amdgcn_raw_buffer_store_b128(o, rws, ho + jj * 512, 0, AUX_SC1);
      }
      asm volatile("s_nop 3" : "+v"(s1), "+v"(s2));
      float x0, x1;
      swap32(s1, x0, x1); s1 = x0 + x1;
      swap32(s2, x0, x1); s2 = x0 + x1;
      if (lane < 32) {
        v2u sv = {asu(s1), asu(s2)};
        __builtin_amdgcn_raw_buffer_store_b64(sv, rws, WS_ST + (((4 * rt + 2 * mh + (tk >> 4)) * 16 + 2 * ct + kh) * 16 + (tk & 15)) * 8, 0, AUX_SC1);
      }
    }
    STAMP(16);
    WAITV(0);
    if (lane == 0) {
      const unsigned old = __hip_atomic_fetch_add(lc + 1, 1u, __ATOMIC_RELAXED, __HIP_MEMORY_SCOPE_WORKGROUP);
      if (old == 3u) __hip_atomic_fetch_add(fl + (FL_C2 + rt * 128) / 4, 1u, RLX_AGENT);
    }
    STAMP(17);
  }
  WAITV(0);
  WAITL0();
  BAR();
  unsigned w4l[2], w4h[2], w4x[2];
  bf16x8 wr[16][2];
  auto wdfrag = [&](int sx, int T) {
    const int sl = wd_slot(sx >> 2), o = ((sx >> 1) & 1) * 4096 + (sx & 1) * 2048;
    return sl >= 131072 ? tr_frag((LAS unsigned char*)(size_t)w4x[T] + o)
         : sl >= 65536 ? tr_frag((LAS unsigned char*)(size_t)w4h[T] + (sl - 65536) + o) : tr_frag((LAS unsigned char*)(size_t)w4l[T] + sl + o);
  };
  auto p4_preread = [&]() {
#pragma unroll
    for (int T = 0; T < 2; ++T) {
      w4l[T] = wt[T] + kh * 8192; w4h[T] = w4l[T] + 65536; w4x[T] = w4l[T] + 131072;
      asm volatile("" : "+v"(w4l[T]), "+v"(w4h[T]), "+v"(w4x[T]));
    }
#pragma unroll
    for (int sx = 0; sx < 16; ++sx)
#pragma unroll
      for (int T = 0; T < 2; ++T) wr[sx][T] = wdfrag(sx, T);
    LC_ADD(3);
  };
  if (has3) {
    {
      f32x4 cs = z4;
      const unsigned wb = (wtr_lane(lane, w & 3) ^ ((w >> 2) << 6)) + (w >> 2) * 4096;
      bf16x8 t[16];
#pragma unroll
      for (int ks = 0; ks < 16; ++ks) t[ks] = tr_frag(L + ks * 8192 + wb);
      SB();
#pragma unroll
      for (int ks = 0; ks < 16; ++ks) cs = mfma32(t[ks], ones, cs);
      if (r == 0) *(LAS f32x4*)(L + P3_CS + (w * 16 + 4 * g) * 4) = cs;
    }
    STAMP(18);
    if (w == 0) {
      for (unsigned spins = 0;;) {
        const bool ok = lane >= 4 || __hip_atomic_load(fl + (FL_C2 + (4 * pn + (lane & 3)) * 128) / 4, RLX_AGENT) >= 8u;
        if (__all(ok)) break;
        if (poll_fail(spins, fl, lane)) break;
      }
      __builtin_amdgcn_fence(__ATOMIC_ACQUIRE, "wavefront");
      if (lane == 0) __hip_atomic_store(lc + 14, 1u, __ATOMIC_RELAXED, __HIP_MEMORY_SCOPE_WORKGROUP);
    } else {
      while (__hip_atomic_load(lc + 14, __ATOMIC_RELAXED, __HIP_MEMORY_SCOPE_WORKGROUP) == 0u) {}
      asm volatile("" ::: "memory");
    }
    STAMP(19);
    const int mt0 = 16 * pn + 2 * w, tk = lane & 31, hf = lane >> 5;
    bf16x8 af[32];
    v2u sp[8];
    const unsigned hsrc = WS_HF + (mt0 + (tk >> 4)) * 16384 + (hf * 16 + (tk & 15)) * 16;
    auto ldh = [&](int s) { af[s] = __builtin_bit_cast(bf16x8, __builtin_amdgcn_raw_buffer_load_b128(rwl, hsrc + (s & 1) * 512, ((((s >> 1) & 8) ^ fl3) | (((s >> 1) + r3) & 7)) * 1024, AUX_SC1)); };
    const unsigned ssrc = WS_ST + (((mt0 + (tk >> 4)) * 16 + 8 * hf) * 16 + (tk & 15)) * 8;
#pragma unroll
    for (int s = 0; s < 8; ++s) ldh(s);
    f32x16 acc[4];
#pragma unroll
    for (int n = 0; n < 4; ++n)
#pragma unroll
      for (int i = 0; i < 16; ++i) acc[n][i] = 0.f;
    unsigned wl[4], wlb[4];
    {
      const int q = (lane & 15) >> 2, pp = lane & 3, bb = (lane >> 4) & 1;
#pragma unroll
      for (int n = 0; n < 4; ++n) {
        const int pos = (2 * n + bb) ^ (((q >> 1) << 1) | (hf << 2) | 0) ^ ((pp >> 1) << 2);
        wl[n] = (pp >> 1) * 4096 + hf * 1024 + q * 128 + pos * 16 + (pp & 1) * 8;
        wlb[n] = wl[n] + GU_B;
        asm volatile("" : "+v"(wl[n]), "+v"(wlb[n]));
      }
    }
    auto rd = [&](unsigned base, int off) { return __builtin_amdgcn_ds_read_tr16_b64_v4i16((LAS bf16x4*)((LAS unsigned char*)(size_t)base + off)); };
#define WR3 6
    bf16x4 wh[WR3][2];
    auto rdM = [&](int M, int h) {
      const int n = M < 64 ? (M & 3) : (M - 64) >> 4, s = M < 64 ? (M >> 2) : ((M - 64) & 15);
      wh[M % WR3][h] = rd(M < 64 ? wl[n] : wlb[n], (s >> 1) * 8192 + (s & 1) * 2048 + h * 512);
    };
    auto mmM = [&](int M) {
      const int n = M < 64 ? (M & 3) : (M - 64) >> 4, s = M < 64 ? (M >> 2) : 16 + ((M - 64) & 15);
      acc[n] = __builtin_amdgcn_mfma_f32_32x32x16_bf16(__builtin_shufflevector(wh[M % WR3][0], wh[M % WR3][1], 0, 1, 2, 3, 4, 5, 6, 7), af[s], acc[n], 0, 0, 0);
    };
#pragma unroll
    for (int M = 0; M < WR3 - 1; ++M) { rdM(M, 0); rdM(M, 1); }
    SB();
    static_for<0, 64>([&](auto MM) {
      constexpr int M = decltype(MM)::value;
      mmM(M);
      rdM(M + WR3 - 1, 0); rdM(M + WR3 - 1, 1);
      if ((M & 1) == 0 && 8 + (M >> 1) < 32) ldh(8 + (M >> 1));
      if (M >= 48 && (M & 1) == 0) sp[(M - 48) >> 1] = __builtin_amdgcn_raw_buffer_load_b64(rwl, ssrc + (((M - 48) >> 1) & 7) * 128, 0, AUX_SC1);
      SB();
    });
    STAMP(20);
    float nmean, c1, c3, irs2;
    {
      float s1 = 0.f, s2 = 0.f;
#pragma unroll
      for (int i = 0; i < 8; ++i) { s1 += asf(sp[i][0]); s2 += asf(sp[i][1]); }
      float a, b;
      swap32(s1, a, b); s1 = a + b;
      swap32(s2, a, b); s2 = a + b;
      const float mn = s1 * (1.f / D_);
      irs2 = fmaxf(s2 * (1.f / D_) - mn * mn, 0.f) + LN_EPS;
      nmean = -mn;
      const float rs = __builtin_amdgcn_rsqf(irs2);
      c1 = -2.302208198f * rs; c3 = -0.1029432397f * rs * rs * rs;
      asm volatile("" : "+v"(nmean), "+v"(c1), "+v"(c3), "+v"(irs2));
    }
    WAITL0();
    BAR();
#pragma unroll
    for (int c = 0; c < 5; ++c) issue_wd(c);
    STAMP(21);
    SB();
    const unsigned actoff = WS_ACT + ((mt0 + (tk >> 4)) * 56 + 2 * fs) * 1024 + (hf * 16 + (tk & 15)) * 16;
    f32x4 cv[4];
    float eG[8], eT[8], eZ[8];
    unsigned ob[4];
    auto epi_step = [&](int n, int k) {
      const int st = k >> 3, v = k & 7, gi = (v >> 2) * 8 + (v & 3), ui = gi + 4;
      if (k == 0) {
        cv[0] = *(LAS f32x4*)(L + P3_CS + (n * 16 + 4 * hf) * 4); cv[1] = *(LAS f32x4*)(L + P3_CS + ((4 + n) * 16 + 4 * hf) * 4);
        cv[2] = *(LAS f32x4*)(L + P3_CS + (n * 16 + 8 + 4 * hf) * 4); cv[3] = *(LAS f32x4*)(L + P3_CS + ((4 + n) * 16 + 8 + 4 * hf) * 4);
      }
      else if (st == 1) eG[v] = fmaf(nmean, cv[(v >> 2) * 2][v & 3], acc[n][gi]);
      else if (st == 2) eT[v] = eG[v] * eG[v];
      else if (st == 3) eT[v] = fmaf(eT[v], c3, c1);
      else if (st == 4) eT[v] = eT[v] * eG[v];
      else if (st == 5) eT[v] = __builtin_amdgcn_exp2f(eT[v]);
      else if (st == 6) eZ[v] = fmaf(nmean, cv[(v >> 2) * 2 + 1][v & 3], acc[n][ui]);
      else if (st == 7) eT[v] = fmaf(eT[v], irs2, irs2);
      else if (st == 8) eZ[v] = eZ[v] * eG[v];
      else if (st == 9) eT[v] = __builtin_amdgcn_rcpf(eT[v]);
      else if (st == 10) eZ[v] = eZ[v] * eT[v];
      else if (k >= 88 && k < 92) ob[k - 88] = pack2(eZ[2 * (k - 88)], eZ[2 * (k - 88) + 1]);
      else if (k == 92) {
        const auto a0 = __builtin_amdgcn_permlane32_swap(ob[0], ob[2], false, false);
        const auto a1 = __builtin_amdgcn_permlane32_swap(ob[1], ob[3], false, false);
        const v4u ov = {a0[0], a1[0], a0[1], a1[1]};
        __builtin_amdgcn_raw_buffer_store_b128(ov, rws, actoff + (n >> 1) * 1024 + (n & 1) * 512, 0, AUX_SC1);
      }
    };
    static_for<64, 128>([&](auto MM) {
      constexpr int M = decltype(MM)::value;
      constexpr int n = (M - 64) >> 4, sl = (M - 64) & 15;
      mmM(M);
      if (M + WR3 - 1 < 128) { rdM(M + WR3 - 1, 0); rdM(M + WR3 - 1, 1); }
      if (n > 0) {
        if (sl == 0) epi_step(n - 1, 0);
        else if (sl <= 14) {
#pragma unroll
          for (int j = 0; j < 6; ++j) epi_step(n - 1, 8 + 6 * (sl - 1) + j);
        } else epi_step(n - 1, 92);
      }
      SB();
    });
    WAITV(3);
    LC_ADD(12);
    STAMP(22);
    epi_step(3, 0);
    static_for<8, 93>([&](auto KK) { epi_step(3, decltype(KK)::value); });
    STAMP(29);
    if (w < 4) { LC_WAITN(12, 8u); p4_preread(); }
    WAITV(0);
    BAR();
    if (tid < 8) __hip_atomic_fetch_add(fl + (FL_C3 + pn * 1024 + tid * 128) / 4, 1u, RLX_AGENT);
    STAMP(30);
  } else {
    issue_wd(0);
    WAITV(0);
    BAR();
    if (w < 4) p4_preread();
  }
  if (w >= 4) {
#pragma unroll
    for (int c = 5; c < 9; ++c) issue_wd4(c, w - 4);
    LC_WAIT(3);
#pragma unroll
    for (int c = 9; c < 13; ++c) issue_wd4(c, w - 4);
    WAITV(16);
    LC_ADD(4);
    WAITV(0);
    LC_ADD(9);
    LC_WAIT(5);
    issue_wd4(13, w - 4);
    WAITV(0);
    LC_ADD(6);
  } else {
    const int ct = c8;
    if (w == 0) {
      for (unsigned spins = 0;;) {
        const bool ok = __hip_atomic_load(fl + (FL_C3 + bx * 1024 + (bi & 7) * 128) / 4, RLX_AGENT) >= 28u;
        if (__all(ok)) break;
        if (poll_fail(spins, fl, lane)) break;
      }
      __builtin_amdgcn_fence(__ATOMIC_ACQUIRE, "wavefront");
      if (lane == 0) __hip_atomic_store(lc + 15, 1u, __ATOMIC_RELAXED, __HIP_MEMORY_SCOPE_WORKGROUP);
      if (bi == 31 && lane == (bx & 1)) __hip_atomic_store(fl + (FL_E + bx * 256) / 4, tagv, RLX_AGENT);
    } else {
      while (__hip_atomic_load(lc + 15, __ATOMIC_RELAXED, __HIP_MEMORY_SCOPE_WORKGROUP) == 0u) {}
      asm volatile("" ::: "memory");
    }
    STAMP(31);
    constexpr int AW = 10;
    bf16x8 a[AW];
    const unsigned asrc = WS_ACT + ((4 * rt + 2 * mh + (tk >> 4)) * 56 + kh * 28) * 1024 + (hf * 16 + (tk & 15)) * 16;
    auto lda = [&](int sx) { const int c = sx >> 2, pc_ = c + r4 >= 14 ? c + r4 - 14 : c + r4;
      a[sx % AW] = __builtin_bit_cast(bf16x8, __builtin_amdgcn_raw_buffer_load_b128(rwl, asrc + ((sx >> 1) & 1) * 1024 + (sx & 1) * 512, pc_ * 2048, AUX_SC1)); };
#pragma unroll
    for (int sx = 0; sx < AW; ++sx) lda(sx);
    f32x16 acc[2];
#pragma unroll
    for (int i = 0; i < 16; ++i) { acc[0][i] = 0.f; acc[1][i] = 0.f; }
#define WR4 4
    bf16x8 wf[WR4][2];
    SB();
    static_for<0, 56>([&](auto SS) {
      constexpr int sx = decltype(SS)::value;
      if (sx == 21 - WR4) LC_WAIT(4);
      if (sx == 37 - WR4) LC_WAIT(9);
      if (sx == 22 - WR4) LC_ADD(5);
      if (sx == 53 - WR4) LC_WAIT(6);
      if (sx + WR4 - 1 >= 16 && sx + WR4 - 1 < 56) { wf[(sx + WR4 - 1) % WR4][0] = wdfrag(sx + WR4 - 1, 0); wf[(sx + WR4 - 1) % WR4][1] = wdfrag(sx + WR4 - 1, 1); }
      acc[0] = __builtin_amdgcn_mfma_f32_32x32x16_bf16(sx < 16 ? wr[sx & 15][0] : wf[sx % WR4][0], a[sx % AW], acc[0], 0, 0, 0);
      acc[1] = __builtin_amdgcn_mfma_f32_32x32x16_bf16(sx < 16 ? wr[sx & 15][1] : wf[sx % WR4][1], a[sx % AW], acc[1], 0, 0, 0);
      if (sx + AW < 56) lda(sx + AW);
      SB();
    });
    STAMP(33);
    LAS f32x4* red = (LAS f32x4*)(L + SM - 16384);
    LC_ADD(7);
    LC_WAIT(7);
#pragma unroll
    for (int j = 0; j < 4; ++j) {
      const f32x16& o = acc[kh ^ 1];
      red[(((mh * 2 + kh) * 4) + j) * 64 + lane] = f32x4{o[4 * j], o[4 * j + 1], o[4 * j + 2], o[4 * j + 3]};
    }
    LC_ADD(8);
    LC_WAIT(8);
    unsigned op[8];
#pragma unroll
    for (int j = 0; j < 4; ++j) {
      const f32x4 o = red[(((mh * 2 + (kh ^ 1)) * 4) + j) * 64 + lane];
      const f32x16& m = acc[kh];
      const unsigned y01 = pack2(m[4 * j] + o[0], m[4 * j + 1] + o[1]), y23 = pack2(m[4 * j + 2] + o[2], m[4 * j + 3] + o[3]);
      op[2 * j] = pack2(bflo(hp[2 * j]) + bflo(y01), bfhi(hp[2 * j]) + bfhi(y01));
      op[2 * j + 1] = pack2(bflo(hp[2 * j + 1]) + bflo(y23), bfhi(hp[2 * j + 1]) + bfhi(y23));
    }
    unsigned short* orow = p.out + (size_t)(64 * rt + 32 * mh + tk) * D_ + 64 * ct + 32 * kh + 8 * hf;
#pragma unroll
    for (int jj = 0; jj < 2; ++jj) {
      const auto a0 = __builtin_amdgcn_permlane32_swap(op[4 * jj], op[4 * jj + 2], false, false);
      const auto a1 = __builtin_amdgcn_permlane32_swap(op[4 * jj + 1], op[4 * jj + 3], false, false);
      const v4u o = {a0[0], a1[0], a0[1], a1[1]};
      *(GAS v4u*)(orow + 16 * jj) = o;
    }
    STAMP(34);
  }
  STAMP_END();
}

#if defined(__HIP_DEVICE_COMPILE__)
#pragma clang attribute pop
#endif
extern "C" void kernel_launch(void* const* d_in, const int* in_sizes, int n_in, void* d_out, int out_size, void* d_ws, size_t ws_size, hipStream_t stream) {
  Params p;
  p.x = (const unsigned short*)d_in[0];
  p.wqkv = (const unsigned short*)d_in[1];
  p.wo = (const unsigned short*)d_in[2];
  p.wg = (const unsigned short*)d_in[4];
  p.wu = (const unsigned short*)d_in[5];
  p.wd = (const unsigned short*)d_in[6];
  p.out = (unsigned short*)d_out;
  p.ws = (unsigned char*)d_ws;
  constexpr int LDS = 160 * 1024;
  (void)hipFuncSetAttribute((const void*)k_fused, hipFuncAttributeMaxDynamicSharedMemorySize, LDS);
  k_fused<<<256, 512, LDS, stream>>>(p);
}
```

```cpp
#include <hip/hip_runtime.h>
#include <hip/hip_bf16.h>

#define GAS __attribute__((address_space(1)))
#define LAS __attribute__((address_space(3)))
typedef short bf16x8 __attribute__((ext_vector_type(8)));
typedef short bf16x4 __attribute__((ext_vector_type(4)));
typedef __bf16 bf2 __attribute__((ext_vector_type(2)));
typedef float f32x4 __attribute__((ext_vector_type(4)));
typedef float f32x2 __attribute__((ext_vector_type(2)));
typedef float f32x16 __attribute__((ext_vector_type(16)));
typedef unsigned v4u __attribute__((ext_vector_type(4)));
typedef unsigned v2u __attribute__((ext_vector_type(2)));
#define DI __device__ __forceinline__

constexpr int S_ = 2048, D_ = 512, F_ = 1792;
constexpr int AUX_SC1 = 16;
constexpr unsigned RSRC3 = 0x00020000u;
constexpr unsigned WS_ATT = 0u;
constexpr unsigned WS_HF = 2u << 20;
constexpr unsigned WS_ACT = 4u << 20;
constexpr unsigned WS_ST = 12u << 20;
constexpr unsigned WS_FL = 13u << 20;
constexpr unsigned FL_E = 0, FL_F1 = 4096, FL_C2 = 16384, FL_C3 = 24576, FL_TMO = 36864;
constexpr unsigned WS_BYTES = 14u << 20;
typedef GAS unsigned gu32;
#define RLX_AGENT __ATOMIC_RELAXED, __HIP_MEMORY_SCOPE_AGENT
constexpr unsigned SPIN_CAP = 1u << 18;
constexpr int WR3 = 6, WR4 = 4;

struct Params {
  const unsigned short* x;
  const unsigned short* wqkv;
  const unsigned short* wo;
  const unsigned short* wg;
  const unsigned short* wu;
  const unsigned short* wd;
  unsigned short* out;
  unsigned char* ws;
};

#if defined(__HIP_DEVICE_COMPILE__)
#pragma clang attribute push (__attribute__((target("no-packed-fp32-ops"))), apply_to = function)
#endif
template <int V> struct IC { static constexpr int value = V; };
template <int A, int B, class F> __device__ __forceinline__ void static_for(F&& f) { if constexpr (A < B) { f(IC<A>{}); static_for<A + 1, B>(f); } }
DI float asf(unsigned u) { return __builtin_bit_cast(float, u); }
DI unsigned asu(float f) { return __builtin_bit_cast(unsigned, f); }
DI f32x4 mfma32(bf16x8 a, bf16x8 b, f32x4 c) { return __builtin_amdgcn_mfma_f32_16x16x32_bf16(a, b, c, 0, 0, 0); }
DI f32x4 mfma16(bf16x4 a, bf16x4 b, f32x4 c) { return __builtin_amdgcn_mfma_f32_16x16x16bf16_1k(a, b, c, 0, 0, 0); }
DI unsigned pack2(float lo, float hi) { bf2 p; p[0] = (__bf16)lo; p[1] = (__bf16)hi; return __builtin_bit_cast(unsigned, p); }
DI float bflo(unsigned u) { return __builtin_bit_cast(float, u << 16); }
DI float bfhi(unsigned u) { return __builtin_bit_cast(float, u & 0xffff0000u); }
DI unsigned ones2() { unsigned o = 0x3F803F80u; asm volatile("" : "+v"(o)); return o; }
DI float dot2(unsigned a, unsigned b, float c) { return __builtin_amdgcn_fdot2_f32_bf16(__builtin_bit_cast(bf2, a), __builtin_bit_cast(bf2, b), c, false); }
DI __amdgpu_buffer_rsrc_t mkrs(const void* p, unsigned bytes) { return __builtin_amdgcn_make_buffer_rsrc((void*)p, 0, bytes, RSRC3); }
DI void dma16(__amdgpu_buffer_rsrc_t rs, unsigned dst, unsigned voff, unsigned soff) {
  asm volatile("s_mov_b32 m0, %0\n\ts_nop 0\n\tbuffer_load_dwordx4 %1, %2, %3 offen lds"
               :: "s"(dst), "v"(voff), "s"(rs), "s"(soff) : "m0", "memory");
}
template <int SS> DI void dma16x4(__amdgpu_buffer_rsrc_t rs, unsigned dst, unsigned v0, unsigned v1, unsigned soff) {
  unsigned t;
  asm volatile("s_mov_b32 m0, %1\n\ts_add_u32 %0, %5, %6\n\t"
               "buffer_load_dwordx4 %2, %4, %5 offen lds\n\t"
               "buffer_load_dwordx4 %3, %4, %0 offen offset:1024 lds\n\ts_add_u32 %0, %0, %6\n\t"
               "buffer_load_dwordx4 %2, %4, %0 offen offset:2048 lds\n\ts_add_u32 %0, %0, %6\n\t"
               "buffer_load_dwordx4 %3, %4, %0 offen offset:3072 lds"
               : "=&s"(t) : "s"(dst), "v"(v0), "v"(v1), "s"(rs), "s"(soff), "n"(SS - 1024) : "m0", "scc", "memory");
}
template <int LS> DI void dma16x3(__amdgpu_buffer_rsrc_t rs, unsigned dst, unsigned v, unsigned soff) {
  asm volatile("s_mov_b32 m0, %0\n\ts_nop 0\n\t"
               "buffer_load_dwordx4 %1, %2, %3 offen lds\n\ts_add_u32 m0, m0, %4\n\ts_nop 0\n\t"
               "buffer_load_dwordx4 %1, %2, %3 offen offset:1024 lds\n\ts_add_u32 m0, m0, %4\n\ts_nop 0\n\t"
               "buffer_load_dwordx4 %1, %2, %3 offen offset:2048 lds"
               :: "s"(dst), "v"(v), "s"(rs), "s"(soff), "n"(LS - 1024) : "m0", "scc", "memory");
}
DI bf16x8 lds_frag(LAS unsigned char* p) { return *(LAS bf16x8*)p; }
DI bf16x8 tr_frag(LAS unsigned char* p) {
  bf16x4 lo = __builtin_amdgcn_ds_read_tr16_b64_v4i16((LAS bf16x4*)p);
  bf16x4 hi = __builtin_amdgcn_ds_read_tr16_b64_v4i16((LAS bf16x4*)(p + 512));
  return __builtin_shufflevector(lo, hi, 0, 1, 2, 3, 4, 5, 6, 7);
}
DI void swap16(float v, float& a, float& b) {
  const unsigned u = asu(v);
  auto r = __builtin_amdgcn_permlane16_swap(u, u, false, false);
  a = asf(r[0]); b = asf(r[1]);
}
DI void swap32(float v, float& a, float& b) {
  const unsigned u = asu(v);
  auto r = __builtin_amdgcn_permlane32_swap(u, u, false, false);
  a = asf(r[0]); b = asf(r[1]);
}
DI float xsum_g(float v) {
  const f32x4 z = {0.f, 0.f, 0.f, 0.f};
  asm volatile("s_nop 3" : "+v"(v));
  return __builtin_amdgcn_mfma_f32_16x16x4f32(1.0f, v, z, 0, 0, 0)[0];
}
DI float xmax_g(float v) {
  float a, b;
  swap16(v, a, b); v = fmaxf(a, b);
  swap32(v, a, b); return fmaxf(a, b);
}
#define WAITV(n) asm volatile("s_waitcnt vmcnt(" #n ")" ::: "memory")
#define WAITL0() asm volatile("s_waitcnt lgkmcnt(0)" ::: "memory")
#define BAR() asm volatile("s_barrier" ::: "memory")
#define SB() __builtin_amdgcn_sched_barrier(0)
#define SG(mask, n) __builtin_amdgcn_sched_group_barrier(mask, n, 0)
#define M_MFMA 0x8
#define M_DSR 0x100
#define M_DSW 0x200
#define M_VALU 0x2
#define M_TRANS 0x400
#define M_VMEMR 0x20
constexpr float LN_EPS = 1e-5f;

DI unsigned wtr_lane(int lane, int nt) {
  const int g = lane >> 4, q = (lane & 15) >> 2, p = lane & 3;
  const int sg = (q >> 1) | ((g & 1) << 1);
  return g * 1024 + q * 128 + ((nt ^ sg) << 5) + p * 8;
}
DI unsigned wdma32_lane(int lane, unsigned pitch) {
  const int r8 = lane >> 3, pos = lane & 7;
  return r8 * pitch + ((pos ^ (((r8 >> 1) & 1) << 2)) << 4);
}
DI unsigned wdma_lane(int lane, unsigned pitch, int odd) {
  const int r8 = lane >> 3, pos = lane & 7;
  return r8 * pitch + ((pos ^ (((r8 >> 1) & 1) << 1) ^ (odd << 2)) << 4);
}

constexpr int P1_X = 0;
constexpr int P1_W = 81920;
constexpr int SM = 155648;
constexpr int P1_PART = SM;
constexpr int P1_CS = SM + 2560;
constexpr int P1_FIN = SM + 4096;
constexpr int LC = SM + 7168;
constexpr int P3_CS = SM;
constexpr int WO_IMG = 65536;
constexpr int RED = 131072;
constexpr int GU_A = 0;
constexpr int GU_B = 65536;
DI constexpr int wd_slot(int c) { return (c % 9) == 0 ? 131072 : ((c % 9) - 1) * 16384; }

DI bool poll_fail(unsigned& spins, gu32* fl, int lane) {
  __builtin_amdgcn_s_sleep(1);
  if (++spins > SPIN_CAP) { if (lane == 0) __hip_atomic_store(fl + FL_TMO / 4, 1u, RLX_AGENT); return true; }
  return false;
}

__global__ __launch_bounds__(512, 2) void k_fused(Params p) {
  extern __shared__ __attribute__((aligned(16))) unsigned char smem[];
  LAS unsigned char* L = (LAS unsigned char*)smem;
  const int tid = threadIdx.x, lane = tid & 63, w = __builtin_amdgcn_readfirstlane(tid >> 6);
  const int bx = blockIdx.x & 7, bi = blockIdx.x >> 3, rt = 4 * bx + (bi >> 3), c8 = bi & 7;
  const int pr = bx >> 1, pc = bx & 1;
  const int r = lane & 15, g = lane >> 4;
  const __amdgpu_buffer_rsrc_t rws = mkrs(p.ws, WS_BYTES);
  gu32* fl = (gu32*)(p.ws + WS_FL);
  const f32x4 z4 = {0.f, 0.f, 0.f, 0.f};
  const bf16x8 ones = {0x3F80, 0x3F80, 0x3F80, 0x3F80, 0x3F80, 0x3F80, 0x3F80, 0x3F80};
  const unsigned o2 = ones2();
  const int rt1 = 8 * pr + (bi >> 2), hg = 4 * pc + (bi & 3);
  const bool has3 = bi < 28;
  const int pn = 2 * pr + (bi >= 14 ? 1 : 0), fs = 14 * pc + (bi >= 14 ? bi - 14 : bi);
  const int r2 = (c8 + 2 * (rt & 3)) & 7, r3 = fs & 7, fl3 = bi >= 14 ? 8 : 0, r4 = (2 * c8 + 3 * (rt & 3)) % 14;
  if (tid < 16) ((LAS unsigned*)(L + LC))[tid] = 0u;
  const unsigned tagv = __hip_atomic_load(fl + (FL_E + (2 * pr + (lane & 1)) * 256) / 4, RLX_AGENT) + 1u;
  if (w == 0) {
    if (hg == 0 && lane == 0) {
      __hip_atomic_store(fl + (FL_C2 + rt1 * 128) / 4, 0u, RLX_AGENT);
      if ((rt1 & 3) == 0) { for (int i = 0; i < 8; ++i) __hip_atomic_store(fl + (FL_C3 + (rt1 >> 2) * 1024 + i * 128) / 4, 0u, RLX_AGENT); }
    }
  }
#define LC_ADD(i) do { WAITL0(); if (lane == 0) __hip_atomic_fetch_add(lc + (i), 1u, __ATOMIC_RELAXED, __HIP_MEMORY_SCOPE_WORKGROUP); } while (0)
#define LC_WAITN(i, n) do { while (__hip_atomic_load(lc + (i), __ATOMIC_RELAXED, __HIP_MEMORY_SCOPE_WORKGROUP) < (n)) __builtin_amdgcn_s_sleep(1); asm volatile("" ::: "memory"); } while (0)
#define LC_WAIT(i) LC_WAITN(i, 4u)
  LAS unsigned* lc = (LAS unsigned*)(L + LC);
  const int tk = lane & 31, hf = lane >> 5, mh = w & 1, kh = (w >> 1) & 1;
  unsigned wt[2];
  {
    const int q = (lane & 15) >> 2, pp = lane & 3, bb = (lane >> 4) & 1;
#pragma unroll
    for (int T = 0; T < 2; ++T) wt[T] = hf * 1024 + q * 128 + (((4 * T + 2 * bb + (pp >> 1)) ^ ((q >> 1) << 2)) << 4) + (pp & 1) * 8;
  }
  bf16x8 wf2[16][2];
  v2u xr[4];
  {
    const int hh = w & 3, rh = w >> 2, rot = (bi + (bi >> 2) + 2 * pr) & 7;
    const __amdgpu_buffer_rsrc_t rx = mkrs(p.x, S_ * D_ * 2), rw = mkrs(p.wqkv, D_ * 3 * D_ * 2);
    const int r8 = lane >> 3, pos = lane & 7;
    const unsigned xv0 = r8 * 1024 + ((pos ^ (r8 >> 1)) << 4), xv1 = xv0 ^ 64;
    auto issue_x = [&](int half) {
      const int c = 4 * half + (w >> 1);
#pragma unroll
      for (int i = 0; i < 5; ++i) {
        const int P = 5 * (w & 1) + i;
        int row = 64 * rt1 - 16 + 8 * P;
        row = row < 0 ? 0 : row;
        dma16(rx, P1_X + (c * 10 + P) * 1024, (P & 1) ? xv1 : xv0, row * 1024 + ((c + rot) & 7) * 128);
      }
    };
    const unsigned wv = wdma_lane(lane, 3072, w & 1);
    auto issue_w = [&](int c) {
      dma16x3<8192>(rw, P1_W + (c % 3) * 24576 + w * 1024, wv, (((c + rot) & 7) * 64 + 8 * w) * 3072 + hg * 128);
    };
    issue_x(0); issue_w(0); issue_w(1);
    f32x4 aq[2] = {z4, z4}, ak[3] = {z4, z4, z4}, av[3] = {z4, z4, z4}, aqh = z4;
    float s1 = 0.f, s2 = 0.f;
    const unsigned xlane = (r >> 3) * 1024 + (r & 7) * 128 + ((g ^ ((r >> 1) & 7)) << 4);
    unsigned xl0 = xlane + 2 * rh * 2048, xl1 = xl0 ^ 64;
    const int stile = rh == 0 ? (hh < 3 ? hh : 0) : (hh < 2 ? 3 + hh : 3);
    unsigned xs0 = xlane + stile * 2048, xs1 = xs0 ^ 64;
    unsigned wlA = wtr_lane(lane, hh) + P1_W, wlB = wlA + 49152;
    unsigned xh0 = xl0 + 40960, xh1 = xl1 + 40960, xsh0 = xs0 + 40960, xsh1 = xs1 + 40960;
    asm volatile("" : "+v"(xl0), "+v"(xl1), "+v"(xs0), "+v"(xs1), "+v"(wlA), "+v"(wlB), "+v"(xh0), "+v"(xh1), "+v"(xsh0), "+v"(xsh1));
    bf16x8 fx[2][4];
    bf16x4 fw[2][3][2];
    auto rd1 = [&](int K, int j) {
      const int c = K >> 1, sx = K & 1, bf = K & 1;
      if (j < 6) {
        const int part = j >> 1, h = j & 1;
        const unsigned base = (c % 3) == 2 ? wlB : wlA;
        const int off = ((c % 3) == 2 ? 0 : (c % 3) * 24576) + part * 8192 + sx * 4096 + h * 512;
        fw[bf][part][h] = __builtin_amdgcn_ds_read_tr16_b64_v4i16((LAS bf16x4*)((LAS unsigned char*)(size_t)base + off));
      } else if (j < 9) {
        const int t = j == 6 ? 1 : j == 7 ? 2 : 0;
        const unsigned base = c < 4 ? (sx ? xl1 : xl0) : (sx ? xh1 : xh0);
        fx[bf][t] = lds_frag((LAS unsigned char*)(size_t)base + (c & 3) * 10240 + t * 2048);
      } else {
        const unsigned base = c < 4 ? (sx ? xs1 : xs0) : (sx ? xsh1 : xsh0);
        fx[bf][3] = lds_frag((LAS unsigned char*)(size_t)base + (c & 3) * 10240);
      }
    };
    constexpr int RORD[10] = {0, 1, 6, 7, 2, 3, 8, 4, 5, 9};
    auto wfrag = [&](int bf, int part) { return __builtin_shufflevector(fw[bf][part][0], fw[bf][part][1], 0, 1, 2, 3, 4, 5, 6, 7); };
    auto mm1 = [&](int K, int j) {
      const int bf = K & 1;
      if (j == 0) aq[0] = mfma32(wfrag(bf, 0), fx[bf][1], aq[0]);
      else if (j == 1) aq[1] = mfma32(wfrag(bf, 0), fx[bf][2], aq[1]);
      else if (j == 2) ak[1] = mfma32(wfrag(bf, 1), fx[bf][1], ak[1]);
      else if (j == 3) ak[2] = mfma32(wfrag(bf, 1), fx[bf][2], ak[2]);
      else if (j == 4) ak[0] = mfma32(wfrag(bf, 1), fx[bf][0], ak[0]);
      else if (j == 5) av[0] = mfma32(fx[bf][0], wfrag(bf, 2), av[0]);
      else if (j == 6) av[1] = mfma32(fx[bf][1], wfrag(bf, 2), av[1]);
      else av[2] = mfma32(fx[bf][2], wfrag(bf, 2), av[2]);
    };
    auto ones_row = [&](int half) {
      if ((w & 1) == 0 && lane < 8) *(LAS v4u*)(L + P1_X + (4 * half + (w >> 1)) * 10240 + lane * 16) = v4u{0x3F803F80u, 0x3F803F80u, 0x3F803F80u, 0x3F803F80u};
    };
    f32x4 b0, b1;
#pragma unroll
    for (int i = 0; i < 4; ++i) {
      const int d0 = 16 + r - (4 * g + i), d1 = r - (4 * g + i);
      b0[i] = (d0 <= 14 && !(d0 & 1)) ? 0.f : -1.0e30f;
      b1[i] = (d1 >= 0 && d1 <= 14 && !(d1 & 1)) ? 0.f : -1.0e30f;
    }
    asm volatile("" : "+v"(b0), "+v"(b1));
    WAITV(3);
    ones_row(0);
    WAITL0();
    BAR();
    issue_w(2);
    v4u wu_[3];
    {
      const unsigned b = blockIdx.x;
      const GAS v4u* q0 = w < 7 ? (const GAS v4u*)p.wg + b * 448 + w * 64 + lane : (const GAS v4u*)p.wo + b * 128 + lane;
      const GAS v4u* q1 = w < 7 ? (const GAS v4u*)p.wu + b * 448 + w * 64 + lane : (const GAS v4u*)p.wo + b * 128 + 64 + lane;
      const GAS v4u* q2 = (const GAS v4u*)p.wd + b * 448 + (w < 7 ? w : 6) * 64 + lane;
      asm volatile("global_load_dwordx4 %0, %3, off\n\tglobal_load_dwordx4 %1, %4, off\n\tglobal_load_dwordx4 %2, %5, off"
                   : "=&v"(wu_[0]), "=&v"(wu_[1]), "=&v"(wu_[2]) : "v"(q0), "v"(q1), "v"(q2) : "memory");
    }
#pragma unroll
    for (int j = 0; j < 10; ++j) rd1(0, RORD[j]);
    SB();
    static_for<0, 16>([&](auto KK) {
      constexpr int K = decltype(KK)::value, c = K >> 1;
      if constexpr ((K & 1) == 1 && c < 7) {
        if (c == 0) WAITV(6);
        else if (c == 1) WAITV(11);
        else if (c == 2) WAITV(8);
        else if (c == 6) WAITV(0);
        else WAITV(3);
        if (c == 2) asm volatile("" :: "v"(wu_[0]), "v"(wu_[1]), "v"(wu_[2]));
        if (c == 3) ones_row(1);
        WAITL0();
        BAR();
        if (c + 3 < 8) issue_w(c + 3);
        if (c == 0) issue_x(1);
        SB();
      }
      static_for<0, 8>([&](auto JJ) {
        constexpr int j = decltype(JJ)::value;
        mm1(K, j);
        if (K + 1 < 16) { rd1(K + 1, RORD[j]); if (j >= 6) rd1(K + 1, RORD[j + 2]); }
        { const v4u u = __builtin_bit_cast(v4u, fx[K & 1][3]);
          if (j & 1) s2 = dot2(u[j >> 1], u[j >> 1], s2); else s1 = dot2(u[j >> 1], o2, s1); }
        SB();
      });
      if (rh == 0) aqh = mfma32(wfrag(K & 1, 0), fx[K & 1][0], aqh);
      SB();
    });
    LAS f32x2* fin = (LAS f32x2*)(L + P1_FIN);
    LAS float* csx = (LAS float*)(L + P1_CS);
    {
      const float a = xsum_g(s1), b = xsum_g(s2);
      const float mean = a * (1.f / D_);
      const float rstd = __builtin_amdgcn_rsqf(fmaxf(b * (1.f / D_) - mean * mean, 0.f) + LN_EPS);
      if (lane < 16 && (rh == 0 ? hh < 3 : hh < 2)) fin[stile * 16 + lane] = f32x2{mean, rstd};
      if (rh == 0) {
        if (r == 0) { *(LAS f32x4*)&csx[hh * 48 + 4 * g] = aqh; *(LAS f32x4*)&csx[hh * 48 + 16 + 4 * g] = ak[0]; }
        if (lane < 16) csx[hh * 48 + 32 + lane] = av[0][0];
      }
    }
    WAITL0();
    BAR();
    {
      const __amdgpu_buffer_rsrc_t rwo = mkrs(p.wo, D_ * D_ * 2);
      const unsigned a0 = wdma32_lane(lane, 1024), a1 = a0;
      dma16x4<8192>(rwo, WO_IMG + w * 8192, a0, a1, (((2 * w) & 8) | ((2 * w + r2) & 7)) * 32768 + c8 * 128);
      dma16x4<8192>(rwo, WO_IMG + w * 8192 + 4096, a0, a1, (((2 * w) & 8) | ((2 * w + 1 + r2) & 7)) * 32768 + c8 * 128);
    }
    const f32x4 cq = *(LAS f32x4*)&csx[hh * 48 + 4 * g], ck = *(LAS f32x4*)&csx[hh * 48 + 16 + 4 * g];
    const float cv0 = csx[hh * 48 + 32 + r];
    bf16x4 Qp[2], Kp[3], Vp[3];
#pragma unroll
    for (int t = 0; t < 3; ++t) {
      const f32x2 st = fin[(2 * rh + t) * 16 + r];
      const f32x4 s01 = *(LAS f32x4*)&fin[(2 * rh + t) * 16 + 4 * g], s23 = *(LAS f32x4*)&fin[(2 * rh + t) * 16 + 4 * g + 2];
      const float nm = -st[0], rs = st[1];
      v2u u;
      u[0] = pack2(fmaf(nm, ck[0], ak[t][0]) * rs, fmaf(nm, ck[1], ak[t][1]) * rs);
      u[1] = pack2(fmaf(nm, ck[2], ak[t][2]) * rs, fmaf(nm, ck[3], ak[t][3]) * rs);
      Kp[t] = __builtin_bit_cast(bf16x4, u);
      if (t >= 1) {
        u[0] = pack2(fmaf(nm, cq[0], aq[t - 1][0]) * rs, fmaf(nm, cq[1], aq[t - 1][1]) * rs);
        u[1] = pack2(fmaf(nm, cq[2], aq[t - 1][2]) * rs, fmaf(nm, cq[3], aq[t - 1][3]) * rs);
        Qp[t - 1] = __builtin_bit_cast(bf16x4, u);
      }
      u[0] = pack2(fmaf(-s01[0], cv0, av[t][0]) * s01[1], fmaf(-s01[2], cv0, av[t][1]) * s01[3]);
      u[1] = pack2(fmaf(-s23[0], cv0, av[t][2]) * s23[1], fmaf(-s23[2], cv0, av[t][3]) * s23[3]);
      Vp[t] = __builtin_bit_cast(bf16x4, u);
    }
    v2u O[2];
#pragma unroll
    for (int qt = 0; qt < 2; ++qt) {
      f32x4 bb = b0;
      if (qt == 0 && rt1 == 0 && rh == 0) bb = f32x4{-1.0e30f, -1.0e30f, -1.0e30f, -1.0e30f};
      const f32x4 S0 = mfma16(Kp[qt], Qp[qt], bb), S1 = mfma16(Kp[qt + 1], Qp[qt], b1);
      float m = fmaxf(fmaxf(fmaxf(S0[0], S0[1]), fmaxf(S0[2], S0[3])), fmaxf(fmaxf(S1[0], S1[1]), fmaxf(S1[2], S1[3])));
      m = xmax_g(m);
      constexpr float SC = 0.25f * 1.4426950408889634f;
      const float ms = -m * SC;
      float v[8];
#pragma unroll
      for (int i = 0; i < 4; ++i) { v[i] = __builtin_amdgcn_exp2f(fmaf(S0[i], SC, ms)); v[4 + i] = __builtin_amdgcn_exp2f(fmaf(S1[i], SC, ms)); }
      float sum = ((v[0] + v[1]) + (v[2] + v[3])) + ((v[4] + v[5]) + (v[6] + v[7]));
      sum = xsum_g(sum);
      const float inv = __builtin_amdgcn_rcpf(sum);
      v2u p0, p1;
      p0[0] = pack2(v[0], v[1]); p0[1] = pack2(v[2], v[3]);
      p1[0] = pack2(v[4], v[5]); p1[1] = pack2(v[6], v[7]);
      f32x4 o = mfma16(Vp[qt], __builtin_bit_cast(bf16x4, p0), z4);
      o = mfma16(Vp[qt + 1], __builtin_bit_cast(bf16x4, p1), o);
      O[qt][0] = pack2(o[0] * inv, o[1] * inv); O[qt][1] = pack2(o[2] * inv, o[3] * inv);
    }
    {
      const auto a = __builtin_amdgcn_permlane16_swap(O[0][0], O[1][0], false, false);
      const auto b = __builtin_amdgcn_permlane16_swap(O[0][1], O[1][1], false, false);
      const v4u o = {a[0], b[0], a[1], b[1]};
      const int head = 4 * hg + hh;
      const unsigned off = WS_ATT + ((4 * rt1 + 2 * rh + (g & 1)) * 16 + (head >> 1)) * 1024 + ((2 * (head & 1) + (g >> 1)) * 16 + r) * 16;
      WAITV(0);
      LC_ADD(10);
      __builtin_amdgcn_raw_buffer_store_b128(o, rws, off, 0, AUX_SC1);
    }
    if (w < 4) {
#pragma unroll
      for (int j = 0; j < 4; ++j) xr[j] = *(const GAS v2u*)(p.x + (size_t)(64 * rt + 32 * mh + tk) * D_ + 64 * c8 + 32 * kh + 8 * j + 4 * hf);
      LC_WAITN(10, 8u);
      unsigned wb[2] = {wt[0] + WO_IMG + kh * 32768, wt[1] + WO_IMG + kh * 32768};
      asm volatile("" : "+v"(wb[0]), "+v"(wb[1]));
#pragma unroll
      for (int sx = 0; sx < 16; ++sx)
#pragma unroll
        for (int T = 0; T < 2; ++T) wf2[sx][T] = tr_frag((LAS unsigned char*)(size_t)wb[T] + (sx >> 1) * 4096 + (sx & 1) * 2048);
      LC_ADD(0);
    }
    WAITV(0);
    {
      unsigned old = 0;
      if (lane == 0) old = __hip_atomic_fetch_add(lc + 11, 1u, __ATOMIC_RELAXED, __HIP_MEMORY_SCOPE_WORKGROUP);
      old = __builtin_amdgcn_readfirstlane(old);
      if (old == 7u && lane == ((rt1 >> 2) & 1)) __hip_atomic_store(fl + (FL_F1 + rt1 * 128) / 4 + hg, tagv, RLX_AGENT);
    }
  }
  const __amdgpu_buffer_rsrc_t rg = mkrs(p.wg, D_ * F_ * 2), ru = mkrs(p.wu, D_ * F_ * 2);
  const unsigned gl0 = wdma_lane(lane, F_ * 2, 0), gl1 = gl0 ^ 64;
  auto issue_gu2 = [&](int half, int ks, int dst) {
    const unsigned so = ((((8 * half) ^ fl3) | ((ks + r3) & 7)) * 32) * (F_ * 2) + fs * 128;
    dma16x4<8 * F_ * 2>(rg, dst + ks * 8192, gl0, gl1, so);
    dma16x4<8 * F_ * 2>(ru, dst + ks * 8192 + 4096, gl1, gl0, so);
  };
  const __amdgpu_buffer_rsrc_t rwd = mkrs(p.wd, F_ * D_ * 2);
  const unsigned dl = wdma32_lane(lane, 1024);
  auto issue_wd4 = [&](int c, int qd) {
    const int pc_ = c + r4 >= 14 ? c + r4 - 14 : c + r4;
    dma16x4<8192>(rwd, wd_slot(c) + qd * 4096, dl, dl, (((qd >> 1) * 28 + 2 * pc_ + (qd & 1)) * 32) * 1024 + c8 * 128);
  };
  auto issue_wd = [&](int c) {
    const int i = w * 2, half = i >> 3, ksl = (i >> 2) & 1, pk = i & 3;
    const int pc_ = c + r4 >= 14 ? c + r4 - 14 : c + r4;
    const unsigned so = ((half * 28 + 2 * pc_ + ksl) * 32 + 8 * pk) * 1024 + c8 * 128;
    dma16(rwd, wd_slot(c) + i * 1024, dl, so);
    dma16(rwd, wd_slot(c) + i * 1024 + 1024, dl, so + 8192);
  };
  unsigned hp[8];
  if (w >= 4) {
    if (has3) {
      LC_WAITN(11, 8u);
      issue_gu2(0, 2 * (w - 4), GU_A); issue_gu2(0, 2 * (w - 4) + 1, GU_A);
      while (__hip_atomic_load(lc, __ATOMIC_RELAXED, __HIP_MEMORY_SCOPE_WORKGROUP) < 4u) __builtin_amdgcn_s_sleep(1);
      issue_gu2(1, 2 * (w - 4), GU_B); issue_gu2(1, 2 * (w - 4) + 1, GU_B);
    } else {
      issue_wd4(1, w - 4); issue_wd4(2, w - 4); issue_wd4(3, w - 4); issue_wd4(4, w - 4);
    }
  } else {
    const int ct = c8;
    if (w == 0) {
      const int frt = lane < 8 ? rt : lane < 12 ? 4 * bx + (lane & 3) : 4 * pn + (lane & 3);
      const unsigned idx = (FL_F1 + frt * 128) / 4 + (lane < 8 ? lane : 0);
      const unsigned tg = __shfl(tagv, (frt >> 2) & 1);
      for (unsigned spins = 0;;) {
        const bool ok = lane >= 16 || __hip_atomic_load(fl + idx, RLX_AGENT) == tg;
        if (__all(ok)) break;
        if (poll_fail(spins, fl, lane)) break;
      }
      __builtin_amdgcn_fence(__ATOMIC_ACQUIRE, "wavefront");
      if (lane == 0) __hip_atomic_store(lc + 13, 1u, __ATOMIC_RELAXED, __HIP_MEMORY_SCOPE_WORKGROUP);
    } else {
      while (__hip_atomic_load(lc + 13, __ATOMIC_RELAXED, __HIP_MEMORY_SCOPE_WORKGROUP) == 0u) {}
      asm volatile("" ::: "memory");
    }
    bf16x8 a[16];
    {
      const unsigned av_ = WS_ATT + ((4 * rt + 2 * mh + (tk >> 4)) * 16 + kh * 8) * 1024 + (hf * 16 + (tk & 15)) * 16;
#pragma unroll
      for (int sx = 0; sx < 16; ++sx)
        a[sx] = __builtin_bit_cast(bf16x8, __builtin_amdgcn_raw_buffer_load_b128(rws, av_ + (sx & 1) * 512, (((sx >> 1) + r2) & 7) * 1024, AUX_SC1));
    }
    SB();
    f32x16 acc[2];
#pragma unroll
    for (int i = 0; i < 16; ++i) { acc[0][i] = 0.f; acc[1][i] = 0.f; }
#pragma unroll
    for (int sx = 0; sx < 16; ++sx)
#pragma unroll
      for (int T = 0; T < 2; ++T) acc[T] = __builtin_amdgcn_mfma_f32_32x32x16_bf16(wf2[sx][T], a[sx], acc[T], 0, 0, 0);
    LAS f32x4* red = (LAS f32x4*)(L + RED);
#pragma unroll
    for (int j = 0; j < 4; ++j) {
      const f32x16& o = acc[kh ^ 1];
      red[(((mh * 2 + kh) * 4) + j) * 64 + lane] = f32x4{o[4 * j], o[4 * j + 1], o[4 * j + 2], o[4 * j + 3]};
    }
    WAITL0();
    if (lane == 0) __hip_atomic_fetch_add(lc + 2, 1u, __ATOMIC_RELAXED, __HIP_MEMORY_SCOPE_WORKGROUP);
    while (__hip_atomic_load(lc + 2, __ATOMIC_RELAXED, __HIP_MEMORY_SCOPE_WORKGROUP) < 4u) {}
    float s1 = 0.f, s2 = 0.f;
#pragma unroll
    for (int j = 0; j < 4; ++j) {
      const f32x4 o = red[(((mh * 2 + (kh ^ 1)) * 4) + j) * 64 + lane];
      const f32x16& m = acc[kh];
      hp[2 * j] = pack2(bflo(xr[j][0]) + (m[4 * j] + o[0]), bfhi(xr[j][0]) + (m[4 * j + 1] + o[1]));
      hp[2 * j + 1] = pack2(bflo(xr[j][1]) + (m[4 * j + 2] + o[2]), bfhi(xr[j][1]) + (m[4 * j + 3] + o[3]));
      s1 = dot2(hp[2 * j], o2, s1); s1 = dot2(hp[2 * j + 1], o2, s1);
      s2 = dot2(hp[2 * j], hp[2 * j], s2); s2 = dot2(hp[2 * j + 1], hp[2 * j + 1], s2);
    }
    {
      const unsigned ho = WS_HF + ((4 * rt + 2 * mh + (tk >> 4)) * 16 + 2 * ct + kh) * 1024 + (hf * 16 + (tk & 15)) * 16;
#pragma unroll
      for (int jj = 0; jj < 2; ++jj) {
        const auto a0 = __builtin_amdgcn_permlane32_swap(hp[4 * jj], hp[4 * jj + 2], false, false);
        const auto a1 = __builtin_amdgcn_permlane32_swap(hp[4 * jj + 1], hp[4 * jj + 3], false, false);
        const v4u o = {a0[0], a1[0], a0[1], a1[1]};
        __builtin_amdgcn_raw_buffer_store_b128(o, rws, ho + jj * 512, 0, AUX_SC1);
      }
      asm volatile("s_nop 3" : "+v"(s1), "+v"(s2));
      float x0, x1;
      swap32(s1, x0, x1); s1 = x0 + x1;
      swap32(s2, x0, x1); s2 = x0 + x1;
      if (lane < 32) {
        v2u sv = {asu(s1), asu(s2)};
        __builtin_amdgcn_raw_buffer_store_b64(sv, rws, WS_ST + (((4 * rt + 2 * mh + (tk >> 4)) * 16 + 2 * ct + kh) * 16 + (tk & 15)) * 8, 0, AUX_SC1);
      }
    }
    WAITV(0);
    if (lane == 0) {
      const unsigned old = __hip_atomic_fetch_add(lc + 1, 1u, __ATOMIC_RELAXED, __HIP_MEMORY_SCOPE_WORKGROUP);
      if (old == 3u) __hip_atomic_fetch_add(fl + (FL_C2 + rt * 128) / 4, 1u, RLX_AGENT);
    }
  }
  WAITV(0);
  WAITL0();
  BAR();
  unsigned w4l[2], w4h[2], w4x[2];
  bf16x8 wr[16][2];
  auto wdfrag = [&](int sx, int T) {
    const int sl = wd_slot(sx >> 2), o = ((sx >> 1) & 1) * 4096 + (sx & 1) * 2048;
    return sl >= 131072 ? tr_frag((LAS unsigned char*)(size_t)w4x[T] + o)
         : sl >= 65536 ? tr_frag((LAS unsigned char*)(size_t)w4h[T] + (sl - 65536) + o) : tr_frag((LAS unsigned char*)(size_t)w4l[T] + sl + o);
  };
  auto p4_preread = [&]() {
#pragma unroll
    for (int T = 0; T < 2; ++T) {
      w4l[T] = wt[T] + kh * 8192; w4h[T] = w4l[T] + 65536; w4x[T] = w4l[T] + 131072;
      asm volatile("" : "+v"(w4l[T]), "+v"(w4h[T]), "+v"(w4x[T]));
    }
#pragma unroll
    for (int sx = 0; sx < 16; ++sx)
#pragma unroll
      for (int T = 0; T < 2; ++T) wr[sx][T] = wdfrag(sx, T);
    LC_ADD(3);
  };
  if (has3) {
    {
      f32x4 cs = z4;
      const unsigned wb = (wtr_lane(lane, w & 3) ^ ((w >> 2) << 6)) + (w >> 2) * 4096;
      bf16x8 t[16];
#pragma unroll
      for (int ks = 0; ks < 16; ++ks) t[ks] = tr_frag(L + ks * 8192 + wb);
      SB();
#pragma unroll
      for (int ks = 0; ks < 16; ++ks) cs = mfma32(t[ks], ones, cs);
      if (r == 0) *(LAS f32x4*)(L + P3_CS + (w * 16 + 4 * g) * 4) = cs;
    }
    if (w == 0) {
      for (unsigned spins = 0;;) {
        const bool ok = lane >= 4 || __hip_atomic_load(fl + (FL_C2 + (4 * pn + (lane & 3)) * 128) / 4, RLX_AGENT) >= 8u;
        if (__all(ok)) break;
        if (poll_fail(spins, fl, lane)) break;
      }
      __builtin_amdgcn_fence(__ATOMIC_ACQUIRE, "wavefront");
      if (lane == 0) __hip_atomic_store(lc + 14, 1u, __ATOMIC_RELAXED, __HIP_MEMORY_SCOPE_WORKGROUP);
    } else {
      while (__hip_atomic_load(lc + 14, __ATOMIC_RELAXED, __HIP_MEMORY_SCOPE_WORKGROUP) == 0u) {}
      asm volatile("" ::: "memory");
    }
    const int mt0 = 16 * pn + 2 * w, tk = lane & 31, hf = lane >> 5;
    bf16x8 af[32];
    v2u sp[8];
    const unsigned hsrc = WS_HF + (mt0 + (tk >> 4)) * 16384 + (hf * 16 + (tk & 15)) * 16;
    auto ldh = [&](int s) { af[s] = __builtin_bit_cast(bf16x8, __builtin_amdgcn_raw_buffer_load_b128(rws, hsrc + (s & 1) * 512, ((((s >> 1) & 8) ^ fl3) | (((s >> 1) + r3) & 7)) * 1024, AUX_SC1)); };
    const unsigned ssrc = WS_ST + (((mt0 + (tk >> 4)) * 16 + 8 * hf) * 16 + (tk & 15)) * 8;
#pragma unroll
    for (int s = 0; s < 8; ++s) ldh(s);
    f32x16 acc[4];
#pragma unroll
    for (int n = 0; n < 4; ++n)
#pragma unroll
      for (int i = 0; i < 16; ++i) acc[n][i] = 0.f;
    unsigned wl[4], wlb[4];
    {
      const int q = (lane & 15) >> 2, pp = lane & 3, bb = (lane >> 4) & 1;
#pragma unroll
      for (int n = 0; n < 4; ++n) {
        const int pos = (2 * n + bb) ^ (((q >> 1) << 1) | (hf << 2) | 0) ^ ((pp >> 1) << 2);
        wl[n] = (pp >> 1) * 4096 + hf * 1024 + q * 128 + pos * 16 + (pp & 1) * 8;
        wlb[n] = wl[n] + GU_B;
        asm volatile("" : "+v"(wl[n]), "+v"(wlb[n]));
      }
    }
    auto rd = [&](unsigned base, int off) { return __builtin_amdgcn_ds_read_tr16_b64_v4i16((LAS bf16x4*)((LAS unsigned char*)(size_t)base + off)); };
    bf16x4 wh[WR3][2];
    auto rdM = [&](int M, int h) {
      const int n = M < 64 ? (M & 3) : (M - 64) >> 4, s = M < 64 ? (M >> 2) : ((M - 64) & 15);
      wh[M % WR3][h] = rd(M < 64 ? wl[n] : wlb[n], (s >> 1) * 8192 + (s & 1) * 2048 + h * 512);
    };
    auto mmM = [&](int M) {
      const int n = M < 64 ? (M & 3) : (M - 64) >> 4, s = M < 64 ? (M >> 2) : 16 + ((M - 64) & 15);
      acc[n] = __builtin_amdgcn_mfma_f32_32x32x16_bf16(__builtin_shufflevector(wh[M % WR3][0], wh[M % WR3][1], 0, 1, 2, 3, 4, 5, 6, 7), af[s], acc[n], 0, 0, 0);
    };
#pragma unroll
    for (int M = 0; M < WR3 - 1; ++M) { rdM(M, 0); rdM(M, 1); }
    SB();
    static_for<0, 64>([&](auto MM) {
      constexpr int M = decltype(MM)::value;
      mmM(M);
      rdM(M + WR3 - 1, 0); rdM(M + WR3 - 1, 1);
      if ((M & 1) == 0 && 8 + (M >> 1) < 32) ldh(8 + (M >> 1));
      if (M >= 48 && (M & 1) == 0) sp[(M - 48) >> 1] = __builtin_amdgcn_raw_buffer_load_b64(rws, ssrc + (((M - 48) >> 1) & 7) * 128, 0, AUX_SC1);
      SB();
    });
    float nmean, c1, c3, irs2;
    {
      float s1 = 0.f, s2 = 0.f;
#pragma unroll
      for (int i = 0; i < 8; ++i) { s1 += asf(sp[i][0]); s2 += asf(sp[i][1]); }
      float a, b;
      swap32(s1, a, b); s1 = a + b;
      swap32(s2, a, b); s2 = a + b;
      const float mn = s1 * (1.f / D_);
      irs2 = fmaxf(s2 * (1.f / D_) - mn * mn, 0.f) + LN_EPS;
      nmean = -mn;
      const float rs = __builtin_amdgcn_rsqf(irs2);
      c1 = -2.302208198f * rs; c3 = -0.1029432397f * rs * rs * rs;
      asm volatile("" : "+v"(nmean), "+v"(c1), "+v"(c3), "+v"(irs2));
    }
    WAITL0();
    BAR();
    SB();
    const unsigned actoff = WS_ACT + ((mt0 + (tk >> 4)) * 56 + 2 * fs) * 1024 + (hf * 16 + (tk & 15)) * 16;
    f32x4 cv[4];
    float eG[8], eT[8], eZ[8];
    unsigned ob[4];
    auto epi_step = [&](int n, int k) {
      const int st = k >> 3, v = k & 7, gi = (v >> 2) * 8 + (v & 3), ui = gi + 4;
      if (k == 0) {
        cv[0] = *(LAS f32x4*)(L + P3_CS + (n * 16 + 4 * hf) * 4); cv[1] = *(LAS f32x4*)(L + P3_CS + ((4 + n) * 16 + 4 * hf) * 4);
        cv[2] = *(LAS f32x4*)(L + P3_CS + (n * 16 + 8 + 4 * hf) * 4); cv[3] = *(LAS f32x4*)(L + P3_CS + ((4 + n) * 16 + 8 + 4 * hf) * 4);
      }
      else if (st == 1) eG[v] = fmaf(nmean, cv[(v >> 2) * 2][v & 3], acc[n][gi]);
      else if (st == 2) eT[v] = eG[v] * eG[v];
      else if (st == 3) eT[v] = fmaf(eT[v], c3, c1);
      else if (st == 4) eT[v] = eT[v] * eG[v];
      else if (st == 5) eT[v] = __builtin_amdgcn_exp2f(eT[v]);
      else if (st == 6) eZ[v] = fmaf(nmean, cv[(v >> 2) * 2 + 1][v & 3], acc[n][ui]);
      else if (st == 7) eT[v] = fmaf(eT[v], irs2, irs2);
      else if (st == 8) eZ[v] = eZ[v] * eG[v];
      else if (st == 9) eT[v] = __builtin_amdgcn_rcpf(eT[v]);
      else if (st == 10) eZ[v] = eZ[v] * eT[v];
      else if (k >= 88 && k < 92) ob[k - 88] = pack2(eZ[2 * (k - 88)], eZ[2 * (k - 88) + 1]);
      else if (k == 92) {
        const auto a0 = __builtin_amdgcn_permlane32_swap(ob[0], ob[2], false, false);
        const auto a1 = __builtin_amdgcn_permlane32_swap(ob[1], ob[3], false, false);
        const v4u ov = {a0[0], a1[0], a0[1], a1[1]};
        __builtin_amdgcn_raw_buffer_store_b128(ov, rws, actoff + (n >> 1) * 1024 + (n & 1) * 512, 0, AUX_SC1);
      }
    };
    static_for<64, 128>([&](auto MM) {
      constexpr int M = decltype(MM)::value;
      constexpr int n = (M - 64) >> 4, sl = (M - 64) & 15;
      mmM(M);
      if (M + WR3 - 1 < 128) { rdM(M + WR3 - 1, 0); rdM(M + WR3 - 1, 1); }
      if (M >= 65 && M < 75 && (M & 1)) issue_wd((M - 65) >> 1);
      if (n > 0) {
        if (sl == 0) epi_step(n - 1, 0);
        else if (sl <= 14) {
#pragma unroll
          for (int j = 0; j < 6; ++j) epi_step(n - 1, 8 + 6 * (sl - 1) + j);
        } else epi_step(n - 1, 92);
      }
      SB();
    });
    epi_step(3, 0);
    static_for<8, 93>([&](auto KK) { epi_step(3, decltype(KK)::value); });
    WAITV(0);
    WAITL0();
    BAR();
    if (tid < 8) __hip_atomic_fetch_add(fl + (FL_C3 + pn * 1024 + tid * 128) / 4, 1u, RLX_AGENT);
    if (w < 4) p4_preread();
  } else {
    issue_wd(0);
    WAITV(0);
    BAR();
    if (w < 4) p4_preread();
  }
  if (w >= 4) {
#pragma unroll
    for (int c = 5; c < 9; ++c) issue_wd4(c, w - 4);
    LC_WAIT(3);
#pragma unroll
    for (int c = 9; c < 13; ++c) issue_wd4(c, w - 4);
    WAITV(16);
    LC_ADD(4);
    WAITV(0);
    LC_ADD(9);
    LC_WAIT(5);
    issue_wd4(13, w - 4);
    WAITV(0);
    LC_ADD(6);
  } else {
    const int ct = c8;
    if (w == 0) {
      for (unsigned spins = 0;;) {
        const bool ok = __hip_atomic_load(fl + (FL_C3 + bx * 1024 + (bi & 7) * 128) / 4, RLX_AGENT) >= 28u;
        if (__all(ok)) break;
        if (poll_fail(spins, fl, lane)) break;
      }
      __builtin_amdgcn_fence(__ATOMIC_ACQUIRE, "wavefront");
      if (lane == 0) __hip_atomic_store(lc + 15, 1u, __ATOMIC_RELAXED, __HIP_MEMORY_SCOPE_WORKGROUP);
      if (bi == 31 && lane == (bx & 1)) __hip_atomic_store(fl + (FL_E + bx * 256) / 4, tagv, RLX_AGENT);
    } else {
      while (__hip_atomic_load(lc + 15, __ATOMIC_RELAXED, __HIP_MEMORY_SCOPE_WORKGROUP) == 0u) {}
      asm volatile("" ::: "memory");
    }
    constexpr int AW = 10;
    bf16x8 a[AW];
    const unsigned asrc = WS_ACT + ((4 * rt + 2 * mh + (tk >> 4)) * 56 + kh * 28) * 1024 + (hf * 16 + (tk & 15)) * 16;
    auto lda = [&](int sx) { const int c = sx >> 2, pc_ = c + r4 >= 14 ? c + r4 - 14 : c + r4;
      a[sx % AW] = __builtin_bit_cast(bf16x8, __builtin_amdgcn_raw_buffer_load_b128(rws, asrc + ((sx >> 1) & 1) * 1024 + (sx & 1) * 512, pc_ * 2048, AUX_SC1)); };
#pragma unroll
    for (int sx = 0; sx < AW; ++sx) lda(sx);
    f32x16 acc[2];
#pragma unroll
    for (int i = 0; i < 16; ++i) { acc[0][i] = 0.f; acc[1][i] = 0.f; }
    bf16x8 wf[WR4][2];
    SB();
    static_for<0, 56>([&](auto SS) {
      constexpr int sx = decltype(SS)::value;
      if (sx == 21 - WR4) LC_WAIT(4);
      if (sx == 37 - WR4) LC_WAIT(9);
      if (sx == 22 - WR4) LC_ADD(5);
      if (sx == 53 - WR4) LC_WAIT(6);
      if (sx + WR4 - 1 >= 16 && sx + WR4 - 1 < 56) { wf[(sx + WR4 - 1) % WR4][0] = wdfrag(sx + WR4 - 1, 0); wf[(sx + WR4 - 1) % WR4][1] = wdfrag(sx + WR4 - 1, 1); }
      acc[0] = __builtin_amdgcn_mfma_f32_32x32x16_bf16(sx < 16 ? wr[sx & 15][0] : wf[sx % WR4][0], a[sx % AW], acc[0], 0, 0, 0);
      acc[1] = __builtin_amdgcn_mfma_f32_32x32x16_bf16(sx < 16 ? wr[sx & 15][1] : wf[sx % WR4][1], a[sx % AW], acc[1], 0, 0, 0);
      if (sx + AW < 56) lda(sx + AW);
      SB();
    });
    LAS f32x4* red = (LAS f32x4*)(L + SM - 16384);
    LC_ADD(7);
    LC_WAIT(7);
#pragma unroll
    for (int j = 0; j < 4; ++j) {
      const f32x16& o = acc[kh ^ 1];
      red[(((mh * 2 + kh) * 4) + j) * 64 + lane] = f32x4{o[4 * j], o[4 * j + 1], o[4 * j + 2], o[4 * j + 3]};
    }
    LC_ADD(8);
    LC_WAIT(8);
    unsigned op[8];
#pragma unroll
    for (int j = 0; j < 4; ++j) {
      const f32x4 o = red[(((mh * 2 + (kh ^ 1)) * 4) + j) * 64 + lane];
      const f32x16& m = acc[kh];
      op[2 * j] = pack2(bflo(hp[2 * j]) + (m[4 * j] + o[0]), bfhi(hp[2 * j]) + (m[4 * j + 1] + o[1]));
      op[2 * j + 1] = pack2(bflo(hp[2 * j + 1]) + (m[4 * j + 2] + o[2]), bfhi(hp[2 * j + 1]) + (m[4 * j + 3] + o[3]));
    }
    unsigned short* orow = p.out + (size_t)(64 * rt + 32 * mh + tk) * D_ + 64 * ct + 32 * kh + 8 * hf;
#pragma unroll
    for (int jj = 0; jj < 2; ++jj) {
      const auto a0 = __builtin_amdgcn_permlane32_swap(op[4 * jj], op[4 * jj + 2], false, false);
      const auto a1 = __builtin_amdgcn_permlane32_swap(op[4 * jj + 1], op[4 * jj + 3], false, false);
      const v4u o = {a0[0], a1[0], a0[1], a1[1]};
      *(GAS v4u*)(orow + 16 * jj) = o;
    }
  }
}

#if defined(__HIP_DEVICE_COMPILE__)
#pragma clang attribute pop
#endif
extern "C" void kernel_launch(void* const* d_in, const int* in_sizes, int n_in, void* d_out, int out_size, void* d_ws, size_t ws_size, hipStream_t stream) {
  Params p;
  p.x = (const unsigned short*)d_in[0];
  p.wqkv = (const unsigned short*)d_in[1];
  p.wo = (const unsigned short*)d_in[2];
  p.wg = (const unsigned short*)d_in[4];
  p.wu = (const unsigned short*)d_in[5];
  p.wd = (const unsigned short*)d_in[6];
  p.out = (unsigned short*)d_out;
  p.ws = (unsigned char*)d_ws;
  constexpr int LDS = 160 * 1024;
  (void)hipFuncSetAttribute((const void*)k_fused, hipFuncAttributeMaxDynamicSharedMemorySize, LDS);
  k_fused<<<256, 512, LDS, stream>>>(p);
}
```
